```python
import torch

def _softmax(x, axis=-1):
    return torch.softmax(x, dim=axis)
def _gelu(x):
    return torch.nn.functional.gelu(x, approximate="tanh")
def _silu(x):
    return torch.nn.functional.silu(x)
def _sigmoid(x):
    return torch.sigmoid(x)
def _layernorm(x, axis=-1, eps=1e-5):
    m = x.mean(dim=axis, keepdim=True)
    v = x.var(dim=axis, keepdim=True, unbiased=False)
    return (x - m) / torch.sqrt(v + eps)
def _scatter_add(tgt, idx, src, axis=0):
    return tgt.clone().index_add_(axis, idx, src)
def _segment_sum(src, seg, n):
    out = torch.zeros((n,) + src.shape[1:], dtype=src.dtype)
    return out.index_add_(0, seg, src)
def _pad(x, pw):
    flat = tuple(v for pair in reversed(pw) for v in pair)
    return torch.nn.functional.pad(x, flat)

kernel_name = 'standalone__paged_block_table_kv_radixattention__decode'

def setup_inputs():

    _w = (lambda a: (lambda t: t.to(torch.bfloat16) if t.is_floating_point() else t)(torch.as_tensor(a)))
    B, H, Hkv, Dh = 2, 32, 16, 128
    Lk = 4096
    bs = 16
    nb = max(2, Lk // bs)
    P = max(1, nb // 2)
    n_phys = P + B * (nb - P)
    shared = torch.arange(P, dtype=torch.int64)
    priv = P + torch.arange(B * (nb - P), dtype=torch.int64).reshape(B, nb - P)
    bt = torch.cat([torch.tile(shared[None, :], (B, 1)), priv], dim=1)
    ctx = nb * bs - (torch.arange(1, B + 1, dtype=torch.int64) * 2654435761) % max(1, (nb * bs - P * bs) // 2)
    return {
        "q": _w((lambda *s: torch.randn(*s, dtype=torch.bfloat16) * 0.8)(B, H, Dh)),
        "k_cache": _w((lambda *s: torch.randn(*s, dtype=torch.bfloat16) * 0.8)(n_phys, bs, Hkv, Dh)),
        "v_cache": _w((lambda *s: torch.randn(*s, dtype=torch.bfloat16) * 0.05)(n_phys, bs, Hkv, Dh)),
        "block_table": _w(bt.to(torch.int64)),
        "ctx_lens": _w(torch.as_tensor(ctx).to(torch.int64)),
    }

def reference(q, k_cache, v_cache, block_table, ctx_lens):
    B, H, Dh = q.shape
    n_phys, bs, Hkv, _ = k_cache.shape
    nb = block_table.shape[1]
    Lk = nb * bs
    rep = H // Hkv
    k = k_cache[block_table].reshape(B, Lk, Hkv, Dh)
    v = v_cache[block_table].reshape(B, Lk, Hkv, Dh)
    k = (lambda x, n, axis: torch.repeat_interleave(x, n, dim=axis))(k, rep, axis=2)
    v = (lambda x, n, axis: torch.repeat_interleave(x, n, dim=axis))(v, rep, axis=2)
    scores = torch.einsum("bhd,bkhd->bhk", q, k) * (Dh ** -0.5)
    pos = torch.arange(Lk)
    valid = pos[None, :] < ctx_lens[:, None]
    scores = torch.where(valid[:, None, :], scores, -1e9)
    p = _softmax(scores, axis=-1)
    return torch.einsum("bhk,bkhd->bhd", p, v)
```

```cpp
#include <hip/hip_runtime.h>
#include <stdint.h>

#define KV_AUX 2

typedef unsigned long long u64;
typedef __attribute__((ext_vector_type(4))) unsigned u32x4;
typedef __attribute__((ext_vector_type(2))) unsigned u32x2;
typedef __attribute__((ext_vector_type(4))) int i32x4;
typedef __attribute__((ext_vector_type(8))) int i32x8;
typedef __attribute__((ext_vector_type(4))) float f32x4;
typedef __attribute__((ext_vector_type(8))) __bf16 bf16x8;
typedef __attribute__((ext_vector_type(4))) short s16x4;
#define GAS __attribute__((address_space(1)))

constexpr int NB = 256;
constexpr int LK = 4096;
constexpr int NSPLIT = 16;
constexpr int WAVES = 8;
constexpr int R = 3;
constexpr float C2 = 0.088388347648318440f * 1.4426950408889634f;
constexpr u64 EPOCH_STEP = 0x9E3779B97F4A7C15ull;
constexpr size_t WS_OG = 4096;
constexpr size_t WS_ML = WS_OG + (size_t)16 * 16 * 4 * 128 * 8;

template <int CTRL> __device__ __forceinline__ float dppf(float x) {
  return __builtin_bit_cast(float, __builtin_amdgcn_mov_dpp(__builtin_bit_cast(int, x), CTRL, 0xf, 0xf, true));
}
__device__ __forceinline__ float xg_max(float x) {
  auto s = __builtin_amdgcn_permlane16_swap(__float_as_uint(x), __float_as_uint(x), false, false);
  x = fmaxf(__uint_as_float(s[0]), __uint_as_float(s[1]));
  auto t = __builtin_amdgcn_permlane32_swap(__float_as_uint(x), __float_as_uint(x), false, false);
  return fmaxf(__uint_as_float(t[0]), __uint_as_float(t[1]));
}
__device__ __forceinline__ float xg_sum(float x) {
  auto s = __builtin_amdgcn_permlane16_swap(__float_as_uint(x), __float_as_uint(x), false, false);
  x = __uint_as_float(s[0]) + __uint_as_float(s[1]);
  auto t = __builtin_amdgcn_permlane32_swap(__float_as_uint(x), __float_as_uint(x), false, false);
  return __uint_as_float(t[0]) + __uint_as_float(t[1]);
}
__device__ __forceinline__ unsigned cvtpk(float lo, float hi) {
  unsigned r; asm("v_cvt_pk_bf16_f32 %0, %1, %2" : "=v"(r) : "v"(lo), "v"(hi)); return r;
}

__global__ void __launch_bounds__(512, 2)
attn_decode(const unsigned short* __restrict__ q, const unsigned short* __restrict__ kc, const unsigned short* __restrict__ vc,
            const int* __restrict__ bt, const int* __restrict__ ctx, unsigned short* __restrict__ out,
            unsigned char* __restrict__ ws, unsigned kv_bytes) {
  __shared__ __attribute__((aligned(16))) float lds[8 * 512 + 2 * 32 + 32];
  float* obuf = lds;
  float* mbuf = lds + 8 * 512;
  float* lbuf = mbuf + 64;

  const int bid = blockIdx.x;
  const int h = (bid >> 3) & 15, js = (bid & 7) | ((bid >> 7) << 3);
  const int tid = threadIdx.x, lane = tid & 63;
  const int wave = __builtin_amdgcn_readfirstlane(tid >> 6);
  const int g = lane >> 4, c = lane & 15;

  const i32x4 bt0 = *reinterpret_cast<const i32x4*>(bt + 4 * lane);
  const i32x4 bt1 = *reinterpret_cast<const i32x4*>(bt + NB + 4 * lane);
  const int ctx0 = ctx[0], ctx1 = ctx[1];
  u32x4 qf[4];
  {
    const int i = c & 3;
    const unsigned short* qp = q + ((i >> 1) * 32 + 2 * h + (i & 1)) * 128 + 8 * g;
#pragma unroll
    for (int kk = 0; kk < 4; ++kk) qf[kk] = *reinterpret_cast<const u32x4*>(qp + 32 * kk);
  }
  GAS u64* ep = (GAS u64*)(ws + h * 256);
  const u64 epoch = __hip_atomic_load(ep, __ATOMIC_RELAXED, __HIP_MEMORY_SCOPE_AGENT) + EPOCH_STEP;

  const bool flat0 = ctx0 <= 0, flat1 = ctx1 <= 0;
  const int c0 = flat0 ? LK : min(ctx0, LK), c1 = flat1 ? LK : min(ctx1, LK);
  const int n0 = (c0 + 15) >> 4, n1 = (c1 + 15) >> 4;
  int S = NB;
  {
    const u64 m0 = __ballot(bt0[0] != bt1[0]), m1 = __ballot(bt0[1] != bt1[1]);
    const u64 m2 = __ballot(bt0[2] != bt1[2]), m3 = __ballot(bt0[3] != bt1[3]);
    const u64 any = m0 | m1 | m2 | m3;
    if (any) {
      const int L = __builtin_ctzll(any);
      const int cmp = ((m0 >> L) & 1) ? 0 : ((m1 >> L) & 1) ? 1 : ((m2 >> L) & 1) ? 2 : 3;
      S = 4 * L + cmp;
    }
  }
  S = min(S, min(n0, n1));
  const int T = n0 + n1 - S;
  const int lo = (T * js) >> 4, hi = (T * (js + 1)) >> 4;
  const int nr = (hi - lo + WAVES * R - 1) / (WAVES * R);

  const __amdgpu_buffer_rsrc_t rk = __builtin_amdgcn_make_buffer_rsrc((void*)kc, 0, kv_bytes, 0x00020000);
  const __amdgpu_buffer_rsrc_t rv = __builtin_amdgcn_make_buffer_rsrc((void*)vc, 0, kv_bytes, 0x00020000);
  const int koff = c * 4096 + g * 16;
  const int voff = g * 16384 + c * 16;
  const bool bsel = (c >> 1) & 1;
  const float sc = (bsel ? flat1 : flat0) ? 0.f : C2;
  const int cl = bsel ? c1 : c0;
  i32x8 btv = {bt0[0], bt0[1], bt0[2], bt0[3], bt1[0], bt1[1], bt1[2], bt1[3]};

  float M = -INFINITY, l = 0.f;
  f32x4 accA = {0.f, 0.f, 0.f, 0.f}, accB = {0.f, 0.f, 0.f, 0.f};

  for (int r0 = 0; r0 < nr; ++r0) {
    u32x4 K[R][4], V[R][4];
    int lim[R];
    int soff[R];
    bool have[R];
#pragma unroll
    for (int r = 0; r < R; ++r) {
      const int t = lo + wave + WAVES * (r0 * R + r);
      have[r] = t < hi;
      const bool b1 = t >= n0;
      const int j = have[r] ? (b1 ? t - n0 + S : t) : 0;
      const int phys = __builtin_amdgcn_readlane(btv[(j & 3) + (b1 ? 4 : 0)], j >> 2);
      soff[r] = phys * 65536 + h * 256;
      const bool cover = b1 ? bsel : (t < S || !bsel);
      lim[r] = (have[r] && cover) ? cl - j * 16 : 0;
    }
#pragma unroll
    for (int r = 0; r < R; ++r)
      if (have[r]) {
#pragma unroll
        for (int kk = 0; kk < 4; ++kk) K[r][kk] = __builtin_amdgcn_raw_buffer_load_b128(rk, koff + kk * 64, soff[r], KV_AUX);
      }
#pragma unroll
    for (int r = 0; r < R; ++r)
      if (have[r]) {
#pragma unroll
        for (int jj = 0; jj < 4; ++jj) V[r][jj] = __builtin_amdgcn_raw_buffer_load_b128(rv, voff, soff[r] + jj * 4096, KV_AUX);
      }
    float s[R][4];
    float mx = -INFINITY;
#pragma unroll
    for (int r = 0; r < R; ++r) {
      f32x4 a = {0.f, 0.f, 0.f, 0.f};
      if (have[r]) {
#pragma unroll
        for (int kk = 0; kk < 4; ++kk)
          a = __builtin_amdgcn_mfma_f32_16x16x32_bf16(__builtin_bit_cast(bf16x8, K[r][kk]), __builtin_bit_cast(bf16x8, qf[kk]), a, 0, 0, 0);
      }
#pragma unroll
      for (int jj = 0; jj < 4; ++jj) {
        s[r][jj] = (4 * g + jj < lim[r]) ? a[jj] * sc : -INFINITY;
        mx = fmaxf(mx, s[r][jj]);
      }
    }
    mx = xg_max(mx);
    float* mb = mbuf + (r0 & 1) * 32;
    if (lane < 4) mb[lane * 8 + wave] = mx;
    __syncthreads();
    {
      const f32x4 a = *reinterpret_cast<const f32x4*>(mb + (c & 3) * 8), b = *reinterpret_cast<const f32x4*>(mb + (c & 3) * 8 + 4);
      mx = fmaxf(fmaxf(fmaxf(a[0], a[1]), fmaxf(a[2], a[3])), fmaxf(fmaxf(b[0], b[1]), fmaxf(b[2], b[3])));
    }
    const float Mn = fmaxf(M, mx), msub = (Mn == -INFINITY) ? 0.f : Mn;
    if (r0 > 0) {
      const float al = __builtin_amdgcn_exp2f(M - msub);
      l *= al;
      const float a0 = dppf<0x00>(al), a1 = dppf<0x55>(al), a2 = dppf<0xAA>(al), a3 = dppf<0xFF>(al);
      accA[0] *= a0; accA[1] *= a1; accA[2] *= a2; accA[3] *= a3;
      accB[0] *= a0; accB[1] *= a1; accB[2] *= a2; accB[3] *= a3;
    }
    M = Mn;
    u32x2 P[R];
#pragma unroll
    for (int r = 0; r < R; ++r) {
      float p[4];
#pragma unroll
      for (int jj = 0; jj < 4; ++jj) { p[jj] = __builtin_amdgcn_exp2f(s[r][jj] - msub); l += p[jj]; }
      P[r][0] = cvtpk(p[0], p[1]); P[r][1] = cvtpk(p[2], p[3]);
    }
#pragma unroll
    for (int r = 0; r < R; ++r)
      if (have[r]) {
#pragma unroll
        for (int w = 0; w < 4; ++w) {
          const bool mine = (c >> 2) == w;
          const u32x2 A = {mine ? P[r][0] : 0u, mine ? P[r][1] : 0u};
          const u32x2 Bl = {__builtin_amdgcn_perm(V[r][1][w], V[r][0][w], 0x05040100u), __builtin_amdgcn_perm(V[r][3][w], V[r][2][w], 0x05040100u)};
          const u32x2 Bh = {__builtin_amdgcn_perm(V[r][1][w], V[r][0][w], 0x07060302u), __builtin_amdgcn_perm(V[r][3][w], V[r][2][w], 0x07060302u)};
          accA = __builtin_amdgcn_mfma_f32_16x16x16bf16_1k(__builtin_bit_cast(s16x4, A), __builtin_bit_cast(s16x4, Bl), accA, 0, 0, 0);
          accB = __builtin_amdgcn_mfma_f32_16x16x16bf16_1k(__builtin_bit_cast(s16x4, A), __builtin_bit_cast(s16x4, Bh), accB, 0, 0, 0);
        }
      }
  }
  l = xg_sum(l);
  if (lane < 4) lbuf[lane * 8 + wave] = l;
  {
    float* ob = obuf + wave * 512 + 8 * c + 2 * (g ^ (c >> 2));
#pragma unroll
    for (int i = 0; i < 4; ++i) *reinterpret_cast<float2*>(ob + i * 128) = make_float2(accA[i], accB[i]);
  }
  __syncthreads();
  const unsigned tagh = (unsigned)(epoch >> 8), tag8 = (unsigned)epoch & 0xffu, tagm = (unsigned)(epoch >> 32) ^ (unsigned)epoch;
  GAS u64* og = (GAS u64*)(ws + WS_OG) + (size_t)h * (16 * 512);
  GAS u64* mlg = (GAS u64*)(ws + WS_ML) + (size_t)h * 128;
  {
    const int i = tid >> 7, d = tid & 127, cc = d >> 3, x = (d >> 1) & 3;
    const float* ob = obuf + i * 128 + 8 * cc + 2 * (x ^ (cc >> 2)) + (d & 1);
    float o = 0.f;
#pragma unroll
    for (int w = 0; w < 8; ++w) o += ob[w * 512];
    const unsigned bits = __float_as_uint(o) + 0x80u;
    const unsigned glo = (bits >> 8) | (tag8 << 24);
    __hip_atomic_store(og + js * 512 + tid, ((u64)tagh << 32) | glo, __ATOMIC_RELAXED, __HIP_MEMORY_SCOPE_AGENT);
    if (wave == 7 && lane < 8) {
      const int row = lane & 3;
      float val = M;
      if (lane >= 4) {
        const f32x4 a = *reinterpret_cast<const f32x4*>(lbuf + row * 8), b = *reinterpret_cast<const f32x4*>(lbuf + row * 8 + 4);
        val = ((a[0] + a[1]) + (a[2] + a[3])) + ((b[0] + b[1]) + (b[2] + b[3]));
      }
      __hip_atomic_store(mlg + row * 32 + (lane >> 2) * 16 + js, ((u64)tagm << 32) | __float_as_uint(val), __ATOMIC_RELAXED, __HIP_MEMORY_SCOPE_AGENT);
    }
  }
  if (wave != 0) return;
  {
    const int rho = js >> 2, qd = js & 3, half = lane >> 5;
    const int pm = (c + 8 * half) & 15;
    GAS u64* om = og + rho * 128 + qd * 32 + (lane & 31) + half * 8 * 512;
    GAS u64* mm = mlg + rho * 32 + pm;
    u64 gv[8], gm, gl;
    bool ok = false;
    for (unsigned spins = 0; spins < (1u << 22); ++spins) {
#pragma unroll
      for (int k = 0; k < 8; ++k) gv[k] = __hip_atomic_load(om + k * 512, __ATOMIC_RELAXED, __HIP_MEMORY_SCOPE_AGENT);
      gm = __hip_atomic_load(mm, __ATOMIC_RELAXED, __HIP_MEMORY_SCOPE_AGENT);
      gl = __hip_atomic_load(mm + 16, __ATOMIC_RELAXED, __HIP_MEMORY_SCOPE_AGENT);
      bool good = (unsigned)(gm >> 32) == tagm && (unsigned)(gl >> 32) == tagm;
#pragma unroll
      for (int k = 0; k < 8; ++k) good = good && (unsigned)(gv[k] >> 32) == tagh && ((unsigned)gv[k] >> 24) == tag8;
      if (__all(good)) { ok = true; break; }
      __builtin_amdgcn_s_sleep(1);
    }
    const float mp = __uint_as_float((unsigned)gm), lp = __uint_as_float((unsigned)gl);
    float Mg = mp;
    Mg = fmaxf(Mg, dppf<0x128>(Mg)); Mg = fmaxf(Mg, dppf<0x124>(Mg)); Mg = fmaxf(Mg, dppf<0x122>(Mg)); Mg = fmaxf(Mg, dppf<0x121>(Mg));
    const float wp = __builtin_amdgcn_exp2f(mp - Mg);
    float L = wp * lp;
    L += dppf<0x128>(L); L += dppf<0x124>(L); L += dppf<0x122>(L); L += dppf<0x121>(L);
    float o = 0.f;
#define ACC(k) o = fmaf(dppf<0x150 + k>(wp), __uint_as_float((unsigned)gv[k] << 8), o);
    ACC(0) ACC(1) ACC(2) ACC(3) ACC(4) ACC(5) ACC(6) ACC(7)
#undef ACC
    auto t = __builtin_amdgcn_permlane32_swap(__float_as_uint(o), __float_as_uint(o), false, false);
    o = __uint_as_float(t[0]) + __uint_as_float(t[1]);
    o = ok ? o / L : __builtin_nanf("");
    const unsigned pk = cvtpk(o, o);
    if (lane < 32) out[((rho >> 1) * 32 + 2 * h + (rho & 1)) * 128 + qd * 32 + lane] = (unsigned short)pk;
    if (js == 0 && lane == 0) __hip_atomic_store(ep, epoch, __ATOMIC_RELAXED, __HIP_MEMORY_SCOPE_AGENT);
  }
}

extern "C" void kernel_launch(void* const* d_in, const int* in_sizes, int n_in, void* d_out, int out_size, void* d_ws, size_t ws_size,
                              hipStream_t stream) {
  const unsigned kv_bytes = (unsigned)in_sizes[1] * 2u;
  attn_decode<<<256, 512, 0, stream>>>((const unsigned short*)d_in[0], (const unsigned short*)d_in[1], (const unsigned short*)d_in[2],
                                       (const int*)d_in[3], (const int*)d_in[4], (unsigned short*)d_out, (unsigned char*)d_ws, kv_bytes);
}
```

```cpp
#define PADT 3000
#include <hip/hip_runtime.h>
#include <stdint.h>

#define KV_AUX 0

typedef unsigned long long u64;
typedef __attribute__((ext_vector_type(4))) unsigned u32x4;
typedef __attribute__((ext_vector_type(2))) unsigned u32x2;
typedef __attribute__((ext_vector_type(4))) int i32x4;
typedef __attribute__((ext_vector_type(8))) int i32x8;
typedef __attribute__((ext_vector_type(4))) float f32x4;
typedef __attribute__((ext_vector_type(8))) __bf16 bf16x8;
typedef __attribute__((ext_vector_type(4))) short s16x4;
#define GAS __attribute__((address_space(1)))

constexpr int NB = 256;
constexpr int LK = 4096;
constexpr int NSPLIT = 16;
constexpr int WAVES = 8;
constexpr int R = 3;
constexpr float C2 = 0.088388347648318440f * 1.4426950408889634f;
constexpr u64 EPOCH_STEP = 0x9E3779B97F4A7C15ull;
constexpr size_t WS_OG = 4096;
constexpr size_t WS_ML = WS_OG + (size_t)16 * 16 * 4 * 128 * 8;

template <int CTRL> __device__ __forceinline__ float dppf(float x) {
  return __builtin_bit_cast(float, __builtin_amdgcn_mov_dpp(__builtin_bit_cast(int, x), CTRL, 0xf, 0xf, true));
}
__device__ __forceinline__ float xg_max(float x) {
  auto s = __builtin_amdgcn_permlane16_swap(__float_as_uint(x), __float_as_uint(x), false, false);
  x = fmaxf(__uint_as_float(s[0]), __uint_as_float(s[1]));
  auto t = __builtin_amdgcn_permlane32_swap(__float_as_uint(x), __float_as_uint(x), false, false);
  return fmaxf(__uint_as_float(t[0]), __uint_as_float(t[1]));
}
__device__ __forceinline__ float xg_sum(float x) {
  auto s = __builtin_amdgcn_permlane16_swap(__float_as_uint(x), __float_as_uint(x), false, false);
  x = __uint_as_float(s[0]) + __uint_as_float(s[1]);
  auto t = __builtin_amdgcn_permlane32_swap(__float_as_uint(x), __float_as_uint(x), false, false);
  return __uint_as_float(t[0]) + __uint_as_float(t[1]);
}
__device__ __forceinline__ unsigned cvtpk(float lo, float hi) {
  unsigned r; asm("v_cvt_pk_bf16_f32 %0, %1, %2" : "=v"(r) : "v"(lo), "v"(hi)); return r;
}

__global__ void __launch_bounds__(512, 2)
attn_decode(const unsigned short* __restrict__ q, const unsigned short* __restrict__ kc, const unsigned short* __restrict__ vc,
            const int* __restrict__ bt, const int* __restrict__ ctx, unsigned short* __restrict__ out,
            unsigned char* __restrict__ ws, unsigned kv_bytes) {
  __shared__ __attribute__((aligned(16))) float lds[8 * 512 + 2 * 32 + 32];
  float* obuf = lds;
  float* mbuf = lds + 8 * 512;
  float* lbuf = mbuf + 64;

  const unsigned long long t_begin = __builtin_amdgcn_s_memrealtime();
  const int bid = blockIdx.x;
  const int h = (bid >> 3) & 15, js = (bid & 7) | ((bid >> 7) << 3);
  const int tid = threadIdx.x, lane = tid & 63;
  const int wave = __builtin_amdgcn_readfirstlane(tid >> 6);
  const int g = lane >> 4, c = lane & 15;

  const i32x4 bt0 = *reinterpret_cast<const i32x4*>(bt + 4 * lane);
  const i32x4 bt1 = *reinterpret_cast<const i32x4*>(bt + NB + 4 * lane);
  const int ctx0 = ctx[0], ctx1 = ctx[1];
  u32x4 qf[4];
  {
    const int i = c & 3;
    const unsigned short* qp = q + ((i >> 1) * 32 + 2 * h + (i & 1)) * 128 + 8 * g;
#pragma unroll
    for (int kk = 0; kk < 4; ++kk) qf[kk] = *reinterpret_cast<const u32x4*>(qp + 32 * kk);
  }
  GAS u64* ep = (GAS u64*)(ws + h * 256);
  const u64 epoch = __hip_atomic_load(ep, __ATOMIC_RELAXED, __HIP_MEMORY_SCOPE_AGENT) + EPOCH_STEP;

  const bool flat0 = ctx0 <= 0, flat1 = ctx1 <= 0;
  const int c0 = flat0 ? LK : min(ctx0, LK), c1 = flat1 ? LK : min(ctx1, LK);
  const int n0 = (c0 + 15) >> 4, n1 = (c1 + 15) >> 4;
  int S = NB;
  {
    const u64 m0 = __ballot(bt0[0] != bt1[0]), m1 = __ballot(bt0[1] != bt1[1]);
    const u64 m2 = __ballot(bt0[2] != bt1[2]), m3 = __ballot(bt0[3] != bt1[3]);
    const u64 any = m0 | m1 | m2 | m3;
    if (any) {
      const int L = __builtin_ctzll(any);
      const int cmp = ((m0 >> L) & 1) ? 0 : ((m1 >> L) & 1) ? 1 : ((m2 >> L) & 1) ? 2 : 3;
      S = 4 * L + cmp;
    }
  }
  S = min(S, min(n0, n1));
  const int T = n0 + n1 - S;
  const int lo = (T * js) >> 4, hi = (T * (js + 1)) >> 4;
  const int nr = (hi - lo + WAVES * R - 1) / (WAVES * R);

  const __amdgpu_buffer_rsrc_t rk = __builtin_amdgcn_make_buffer_rsrc((void*)kc, 0, kv_bytes, 0x00020000);
  const __amdgpu_buffer_rsrc_t rv = __builtin_amdgcn_make_buffer_rsrc((void*)vc, 0, kv_bytes, 0x00020000);
  const int koff = c * 4096 + g * 16;
  const int voff = g * 16384 + c * 16;
  const bool bsel = (c >> 1) & 1;
  const float sc = (bsel ? flat1 : flat0) ? 0.f : C2;
  const int cl = bsel ? c1 : c0;
  i32x8 btv = {bt0[0], bt0[1], bt0[2], bt0[3], bt1[0], bt1[1], bt1[2], bt1[3]};

  float M = -INFINITY, l = 0.f;
  f32x4 accA = {0.f, 0.f, 0.f, 0.f}, accB = {0.f, 0.f, 0.f, 0.f};

  for (int r0 = 0; r0 < nr; ++r0) {
    u32x4 K[R][4], V[R][4];
    int lim[R];
    int soff[R];
    bool have[R];
#pragma unroll
    for (int r = 0; r < R; ++r) {
      const int t = lo + wave + WAVES * (r0 * R + r);
      have[r] = t < hi;
      const bool b1 = t >= n0;
      const int j = have[r] ? (b1 ? t - n0 + S : t) : 0;
      const int phys = __builtin_amdgcn_readlane(btv[(j & 3) + (b1 ? 4 : 0)], j >> 2);
      soff[r] = phys * 65536 + h * 256;
      const bool cover = b1 ? bsel : (t < S || !bsel);
      lim[r] = (have[r] && cover) ? cl - j * 16 : 0;
    }
#pragma unroll
    for (int r = 0; r < R; ++r)
      if (have[r]) {
#pragma unroll
        for (int kk = 0; kk < 4; ++kk) K[r][kk] = __builtin_amdgcn_raw_buffer_load_b128(rk, koff + kk * 64, soff[r], KV_AUX);
      }
#pragma unroll
    for (int r = 0; r < R; ++r)
      if (have[r]) {
#pragma unroll
        for (int jj = 0; jj < 4; ++jj) V[r][jj] = __builtin_amdgcn_raw_buffer_load_b128(rv, voff, soff[r] + jj * 4096, KV_AUX);
      }
    float s[R][4];
    float mx = -INFINITY;
#pragma unroll
    for (int r = 0; r < R; ++r) {
      f32x4 a = {0.f, 0.f, 0.f, 0.f};
      if (have[r]) {
#pragma unroll
        for (int kk = 0; kk < 4; ++kk)
          a = __builtin_amdgcn_mfma_f32_16x16x32_bf16(__builtin_bit_cast(bf16x8, K[r][kk]), __builtin_bit_cast(bf16x8, qf[kk]), a, 0, 0, 0);
      }
#pragma unroll
      for (int jj = 0; jj < 4; ++jj) {
        s[r][jj] = (4 * g + jj < lim[r]) ? a[jj] * sc : -INFINITY;
        mx = fmaxf(mx, s[r][jj]);
      }
    }
    mx = xg_max(mx);
    float* mb = mbuf + (r0 & 1) * 32;
    if (lane < 4) mb[lane * 8 + wave] = mx;
    __syncthreads();
    {
      const f32x4 a = *reinterpret_cast<const f32x4*>(mb + (c & 3) * 8), b = *reinterpret_cast<const f32x4*>(mb + (c & 3) * 8 + 4);
      mx = fmaxf(fmaxf(fmaxf(a[0], a[1]), fmaxf(a[2], a[3])), fmaxf(fmaxf(b[0], b[1]), fmaxf(b[2], b[3])));
    }
    const float Mn = fmaxf(M, mx), msub = (Mn == -INFINITY) ? 0.f : Mn;
    if (r0 > 0) {
      const float al = __builtin_amdgcn_exp2f(M - msub);
      l *= al;
      const float a0 = dppf<0x00>(al), a1 = dppf<0x55>(al), a2 = dppf<0xAA>(al), a3 = dppf<0xFF>(al);
      accA[0] *= a0; accA[1] *= a1; accA[2] *= a2; accA[3] *= a3;
      accB[0] *= a0; accB[1] *= a1; accB[2] *= a2; accB[3] *= a3;
    }
    M = Mn;
    u32x2 P[R];
#pragma unroll
    for (int r = 0; r < R; ++r) {
      float p[4];
#pragma unroll
      for (int jj = 0; jj < 4; ++jj) { p[jj] = __builtin_amdgcn_exp2f(s[r][jj] - msub); l += p[jj]; }
      P[r][0] = cvtpk(p[0], p[1]); P[r][1] = cvtpk(p[2], p[3]);
    }
#pragma unroll
    for (int r = 0; r < R; ++r)
      if (have[r]) {
#pragma unroll
        for (int w = 0; w < 4; ++w) {
          const bool mine = (c >> 2) == w;
          const u32x2 A = {mine ? P[r][0] : 0u, mine ? P[r][1] : 0u};
          const u32x2 Bl = {__builtin_amdgcn_perm(V[r][1][w], V[r][0][w], 0x05040100u), __builtin_amdgcn_perm(V[r][3][w], V[r][2][w], 0x05040100u)};
          const u32x2 Bh = {__builtin_amdgcn_perm(V[r][1][w], V[r][0][w], 0x07060302u), __builtin_amdgcn_perm(V[r][3][w], V[r][2][w], 0x07060302u)};
          accA = __builtin_amdgcn_mfma_f32_16x16x16bf16_1k(__builtin_bit_cast(s16x4, A), __builtin_bit_cast(s16x4, Bl), accA, 0, 0, 0);
          accB = __builtin_amdgcn_mfma_f32_16x16x16bf16_1k(__builtin_bit_cast(s16x4, A), __builtin_bit_cast(s16x4, Bh), accB, 0, 0, 0);
        }
      }
  }
  l = xg_sum(l);
  if (lane < 4) lbuf[lane * 8 + wave] = l;
  {
    float* ob = obuf + wave * 512 + 8 * c + 2 * (g ^ (c >> 2));
#pragma unroll
    for (int i = 0; i < 4; ++i) *reinterpret_cast<float2*>(ob + i * 128) = make_float2(accA[i], accB[i]);
  }
  __syncthreads();
  const unsigned tagh = (unsigned)(epoch >> 8), tag8 = (unsigned)epoch & 0xffu, tagm = (unsigned)(epoch >> 32) ^ (unsigned)epoch;
  GAS u64* og = (GAS u64*)(ws + WS_OG) + (size_t)h * (16 * 512);
  GAS u64* mlg = (GAS u64*)(ws + WS_ML) + (size_t)h * 128;
  {
    const int i = tid >> 7, d = tid & 127, cc = d >> 3, x = (d >> 1) & 3;
    const float* ob = obuf + i * 128 + 8 * cc + 2 * (x ^ (cc >> 2)) + (d & 1);
    float o = 0.f;
#pragma unroll
    for (int w = 0; w < 8; ++w) o += ob[w * 512];
    const unsigned bits = __float_as_uint(o) + 0x80u;
    const unsigned glo = (bits >> 8) | (tag8 << 24);
    __hip_atomic_store(og + js * 512 + tid, ((u64)tagh << 32) | glo, __ATOMIC_RELAXED, __HIP_MEMORY_SCOPE_AGENT);
    if (wave == 7 && lane < 8) {
      const int row = lane & 3;
      float val = M;
      if (lane >= 4) {
        const f32x4 a = *reinterpret_cast<const f32x4*>(lbuf + row * 8), b = *reinterpret_cast<const f32x4*>(lbuf + row * 8 + 4);
        val = ((a[0] + a[1]) + (a[2] + a[3])) + ((b[0] + b[1]) + (b[2] + b[3]));
      }
      __hip_atomic_store(mlg + row * 32 + (lane >> 2) * 16 + js, ((u64)tagm << 32) | __float_as_uint(val), __ATOMIC_RELAXED, __HIP_MEMORY_SCOPE_AGENT);
    }
  }
  if (wave != 0) return;
  {
    const int rho = js >> 2, qd = js & 3, half = lane >> 5;
    const int pm = (c + 8 * half) & 15;
    GAS u64* om = og + rho * 128 + qd * 32 + (lane & 31) + half * 8 * 512;
    GAS u64* mm = mlg + rho * 32 + pm;
    u64 gv[8], gm, gl;
    bool ok = false;
    for (unsigned spins = 0; spins < (1u << 22); ++spins) {
#pragma unroll
      for (int k = 0; k < 8; ++k) gv[k] = __hip_atomic_load(om + k * 512, __ATOMIC_RELAXED, __HIP_MEMORY_SCOPE_AGENT);
      gm = __hip_atomic_load(mm, __ATOMIC_RELAXED, __HIP_MEMORY_SCOPE_AGENT);
      gl = __hip_atomic_load(mm + 16, __ATOMIC_RELAXED, __HIP_MEMORY_SCOPE_AGENT);
      bool good = (unsigned)(gm >> 32) == tagm && (unsigned)(gl >> 32) == tagm;
#pragma unroll
      for (int k = 0; k < 8; ++k) good = good && (unsigned)(gv[k] >> 32) == tagh && ((unsigned)gv[k] >> 24) == tag8;
      if (__all(good)) { ok = true; break; }
      __builtin_amdgcn_s_sleep(1);
    }
    const float mp = __uint_as_float((unsigned)gm), lp = __uint_as_float((unsigned)gl);
    float Mg = mp;
    Mg = fmaxf(Mg, dppf<0x128>(Mg)); Mg = fmaxf(Mg, dppf<0x124>(Mg)); Mg = fmaxf(Mg, dppf<0x122>(Mg)); Mg = fmaxf(Mg, dppf<0x121>(Mg));
    const float wp = __builtin_amdgcn_exp2f(mp - Mg);
    float L = wp * lp;
    L += dppf<0x128>(L); L += dppf<0x124>(L); L += dppf<0x122>(L); L += dppf<0x121>(L);
    float o = 0.f;
#define ACC(k) o = fmaf(dppf<0x150 + k>(wp), __uint_as_float((unsigned)gv[k] << 8), o);
    ACC(0) ACC(1) ACC(2) ACC(3) ACC(4) ACC(5) ACC(6) ACC(7)
#undef ACC
    auto t = __builtin_amdgcn_permlane32_swap(__float_as_uint(o), __float_as_uint(o), false, false);
    o = __uint_as_float(t[0]) + __uint_as_float(t[1]);
    o = ok ? o / L : __builtin_nanf("");
    const unsigned pk = cvtpk(o, o);
    if (lane < 32) out[((rho >> 1) * 32 + 2 * h + (rho & 1)) * 128 + qd * 32 + lane] = (unsigned short)pk;
    if (js == 0 && lane == 0) __hip_atomic_store(ep, epoch, __ATOMIC_RELAXED, __HIP_MEMORY_SCOPE_AGENT);
    while (__builtin_amdgcn_s_memrealtime() - t_begin < PADT) __builtin_amdgcn_s_sleep(1);
  }
}

extern "C" void kernel_launch(void* const* d_in, const int* in_sizes, int n_in, void* d_out, int out_size, void* d_ws, size_t ws_size,
                              hipStream_t stream) {
  const unsigned kv_bytes = (unsigned)in_sizes[1] * 2u;
  attn_decode<<<256, 512, 0, stream>>>((const unsigned short*)d_in[0], (const unsigned short*)d_in[1], (const unsigned short*)d_in[2],
                                       (const int*)d_in[3], (const int*)d_in[4], (unsigned short*)d_out, (unsigned char*)d_ws, kv_bytes);
}
```

```cpp
#include <hip/hip_runtime.h>
#include <stdint.h>

__global__ void __launch_bounds__(512, 2)
attn_decode(const void* kc, unsigned kb0, unsigned kf0, const void* vc, unsigned kb1, unsigned kf1, const void* q, const void* bt, const void* ctx, void* out, void* ws) {
#if defined(__HIP_DEVICE_COMPILE__)
  __shared__ __attribute__((aligned(16))) unsigned char lds[147712];
  asm volatile(
      "s_load_dwordx16 s[4:19], %0, 0x0\n"
      "s_load_dwordx2 s[20:21], %0, 0x40\n"
      "v_and_b32 v1, 63, %2\n"
      "v_readfirstlane_b32 s28, %2\n"
      "v_lshrrev_b32 v2, 4, v1\n"
      "v_and_b32 v3, 15, %2\n"
      "s_lshr_b32 s28, s28, 6\n"
      "s_bfe_u32 s29, %1, 0x40003\n"
      "s_and_b32 s30, %1, 7\n"
      "s_lshr_b32 s26, %1, 7\n"
      "s_lshl3_add_u32 s30, s26, s30\n"
      "s_lshl4_add_u32 s31, s28, s30\n"
      "s_lshl_b32 s33, s29, 8\n"
      "s_lshl_b32 s34, s28, 14\n"
      "s_lshl_b32 s35, s31, 2\n"
      "s_lshl_b32 s55, s31, 4\n"
      "v_and_b32 v195, 2, v3\n"
      "v_and_b32 v194, 1, v3\n"
      "v_lshlrev_b32 v195, 12, v195\n"
      "v_lshl_or_b32 v195, v194, 8, v195\n"
      "v_lshl_or_b32 v195, v2, 4, v195\n"
      "v_mov_b32 v196, 0\n"
      "v_xor_b32 v190, v3, v2\n"
      "v_lshlrev_b32 v191, 12, v2\n"
      "v_lshl_add_u32 v4, v190, 4, v191\n"
      "s_lshl_b32 s64, s29, 9\n"
      "s_waitcnt lgkmcnt(0)\n"
      "s_add_u32 s14, s14, s35\n"
      "s_addc_u32 s15, s15, 0\n"
      "s_load_dword s36, s[14:15], 0x0\n"
      "s_load_dword s38, s[14:15], 0x400\n"
      "s_load_dwordx2 s[40:41], s[16:17], 0x0\n"
      "s_load_dword s37, s[14:15], 0x200\n"
      "s_load_dword s39, s[14:15], 0x600\n"
      "s_lshr_b32 s70, s4, 8\n"
      "s_add_u32 s70, s70, s29\n"
      "s_and_b32 s70, s70, 3\n"
      "s_cmp_eq_u32 s70, 1\n"
      "s_cselect_b32 s70, 1, 0\n"
      "s_add_u32 s12, s12, s64\n"
      "s_addc_u32 s13, s13, 0\n"
      "s_add_u32 s66, s20, s33\n"
      "s_addc_u32 s67, s21, 0\n"
      "global_load_dwordx4 v[16:19], v195, s[12:13]\n"
      "global_load_dwordx4 v[20:23], v195, s[12:13] offset:64\n"
      "global_load_dwordx4 v[24:27], v195, s[12:13] offset:128\n"
      "global_load_dwordx4 v[28:31], v195, s[12:13] offset:192\n"
      "global_load_dword v184, v196, s[66:67] sc1\n"
      "v_xor_b32 v5, 64, v4\n"
      "v_add_u32 v5, 15360, v5\n"
      "v_xor_b32 v6, 128, v4\n"
      "v_add_u32 v6, 30720, v6\n"
      "v_xor_b32 v7, 192, v4\n"
      "v_add_u32 v7, 46080, v7\n"
      "v_lshlrev_b32 v193, 4, v3\n"
      "v_lshl_add_u32 v8, v2, 14, v193\n"
      "v_add_u32 v9, 0x1000, v8\n"
      "v_add_u32 v10, 0x2000, v8\n"
      "v_add_u32 v11, 0x3000, v8\n"
      "s_mov_b32 s54, 0\n"
      "s_waitcnt lgkmcnt(0)\n"
      "s_min_i32 s42, s40, 0x1000\n"
      "s_cmp_lt_i32 s40, 1\n"
      "s_cselect_b32 s42, 0x1000, s42\n"
      "s_cselect_b32 s44, 1, 0\n"
      "s_min_i32 s43, s41, 0x1000\n"
      "s_cmp_lt_i32 s41, 1\n"
      "s_cselect_b32 s43, 0x1000, s43\n"
      "s_cselect_b32 s45, 1, 0\n"
      "s_sub_i32 s46, s42, s55\n"
      "s_sub_i32 s47, s43, s55\n"
      "s_max_i32 s64, s46, s47\n"
      "s_cmp_gt_i32 s64, 0\n"
      "s_cbranch_scc0 L_nowork_%=\n"
      "s_cmp_gt_i32 s46, 0\n"
      "s_cselect_b32 s50, s36, s38\n"
      "s_lshl_b32 s50, s50, 16\n"
      "s_mov_b32 m0, s34\n"
      "s_or_b32 s50, s50, s33\n"
      "buffer_load_dwordx4 v4, s[4:7], s50 offen nt lds\n"
      "buffer_load_dwordx4 v5, s[4:7], s50 offen offset:1024 nt lds\n"
      "buffer_load_dwordx4 v6, s[4:7], s50 offen offset:2048 nt lds\n"
      "buffer_load_dwordx4 v7, s[4:7], s50 offen offset:3072 nt lds\n"
      "s_sub_i32 s48, s46, 0x800\n"
      "s_sub_i32 s49, s47, 0x800\n"
      "s_min_i32 s64, s46, s47\n"
      "s_cmp_lg_u32 s36, s38\n"
      "s_cselect_b32 s64, s64, 0\n"
      "s_cmp_gt_i32 s64, 0\n"
      "s_cselect_b32 s65, 1, 0\n"
      "s_max_i32 s64, s48, s49\n"
      "s_cmp_gt_i32 s64, 0\n"
      "s_cselect_b32 s66, 1, 0\n"
      "s_min_i32 s64, s48, s49\n"
      "s_cmp_lg_u32 s37, s39\n"
      "s_cselect_b32 s64, s64, 0\n"
      "s_cmp_gt_i32 s64, 0\n"
      "s_cselect_b32 s67, 1, 0\n"
      "s_add_i32 s54, s65, s66\n"
      "s_add_i32 s54, s54, s67\n"
      "s_add_i32 s54, s54, 1\n"
      "s_cmp_gt_i32 s48, 0\n"
      "s_cselect_b32 s68, s37, s39\n"
      "s_cmp_lg_u32 s66, 0\n"
      "s_cselect_b32 s51, s68, s38\n"
      "s_cmp_lg_u32 s65, 0\n"
      "s_cselect_b32 s52, s38, s39\n"
      "s_lshl_b32 s51, s51, 16\n"
      "s_lshl_b32 s52, s52, 16\n"
      "s_lshl_b32 s53, s39, 16\n"
      "s_or_b32 s51, s51, s33\n"
      "s_or_b32 s52, s52, s33\n"
      "s_or_b32 s53, s53, s33\n"
      "s_cmp_lt_u32 s54, 2\n"
      "s_cbranch_scc1 L_kdone_%=\n"
      "s_add_u32 m0, s34, 4096\n"
      "s_nop 0\n"
      "s_cmp_lg_u32 s70, 0\n"
      "s_cbranch_scc1 L_tw1_%=\n"
      "s_waitcnt vmcnt(3)\n"
      "L_tw1_%=:\n"
      "buffer_load_dwordx4 v4, s[4:7], s51 offen nt lds\n"
      "s_cmp_lg_u32 s70, 0\n"
      "s_cbranch_scc1 L_tw2_%=\n"
      "s_waitcnt vmcnt(3)\n"
      "L_tw2_%=:\n"
      "buffer_load_dwordx4 v5, s[4:7], s51 offen offset:1024 nt lds\n"
      "s_cmp_lg_u32 s70, 0\n"
      "s_cbranch_scc1 L_tw3_%=\n"
      "s_waitcnt vmcnt(3)\n"
      "L_tw3_%=:\n"
      "buffer_load_dwordx4 v6, s[4:7], s51 offen offset:2048 nt lds\n"
      "s_cmp_lg_u32 s70, 0\n"
      "s_cbranch_scc1 L_tw4_%=\n"
      "s_waitcnt vmcnt(3)\n"
      "L_tw4_%=:\n"
      "buffer_load_dwordx4 v7, s[4:7], s51 offen offset:3072 nt lds\n"
      "s_cmp_lt_u32 s54, 3\n"
      "s_cbranch_scc1 L_kdone_%=\n"
      "s_add_u32 m0, s34, 8192\n"
      "s_nop 0\n"
      "s_cmp_lg_u32 s70, 0\n"
      "s_cbranch_scc1 L_tw5_%=\n"
      "s_waitcnt vmcnt(3)\n"
      "L_tw5_%=:\n"
      "buffer_load_dwordx4 v4, s[4:7], s52 offen nt lds\n"
      "s_cmp_lg_u32 s70, 0\n"
      "s_cbranch_scc1 L_tw6_%=\n"
      "s_waitcnt vmcnt(3)\n"
      "L_tw6_%=:\n"
      "buffer_load_dwordx4 v5, s[4:7], s52 offen offset:1024 nt lds\n"
      "s_cmp_lg_u32 s70, 0\n"
      "s_cbranch_scc1 L_tw7_%=\n"
      "s_waitcnt vmcnt(3)\n"
      "L_tw7_%=:\n"
      "buffer_load_dwordx4 v6, s[4:7], s52 offen offset:2048 nt lds\n"
      "s_cmp_lg_u32 s70, 0\n"
      "s_cbranch_scc1 L_tw8_%=\n"
      "s_waitcnt vmcnt(3)\n"
      "L_tw8_%=:\n"
      "buffer_load_dwordx4 v7, s[4:7], s52 offen offset:3072 nt lds\n"
      "s_cmp_lt_u32 s54, 4\n"
      "s_cbranch_scc1 L_kdone_%=\n"
      "s_add_u32 m0, s34, 12288\n"
      "s_nop 0\n"
      "s_cmp_lg_u32 s70, 0\n"
      "s_cbranch_scc1 L_tw9_%=\n"
      "s_waitcnt vmcnt(3)\n"
      "L_tw9_%=:\n"
      "buffer_load_dwordx4 v4, s[4:7], s53 offen nt lds\n"
      "s_cmp_lg_u32 s70, 0\n"
      "s_cbranch_scc1 L_tw10_%=\n"
      "s_waitcnt vmcnt(3)\n"
      "L_tw10_%=:\n"
      "buffer_load_dwordx4 v5, s[4:7], s53 offen offset:1024 nt lds\n"
      "s_cmp_lg_u32 s70, 0\n"
      "s_cbranch_scc1 L_tw11_%=\n"
      "s_waitcnt vmcnt(3)\n"
      "L_tw11_%=:\n"
      "buffer_load_dwordx4 v6, s[4:7], s53 offen offset:2048 nt lds\n"
      "s_cmp_lg_u32 s70, 0\n"
      "s_cbranch_scc1 L_tw12_%=\n"
      "s_waitcnt vmcnt(3)\n"
      "L_tw12_%=:\n"
      "buffer_load_dwordx4 v7, s[4:7], s53 offen offset:3072 nt lds\n"
      "L_kdone_%=:\n"
      "s_mov_b32 s71, s54\n"
      "s_mov_b32 s56, s46\n"
      "s_cmp_lg_u32 s36, s38\n"
      "s_cselect_b32 s64, 0, s47\n"
      "s_cmp_gt_i32 s46, 0\n"
      "s_cselect_b32 s60, s64, s47\n"
      "s_cmp_lg_u32 s37, s39\n"
      "s_cselect_b32 s64, 0, s49\n"
      "s_cmp_gt_i32 s48, 0\n"
      "s_cselect_b32 s64, s64, s49\n"
      "s_cmp_lg_u32 s66, 0\n"
      "s_cselect_b32 s57, s48, 0\n"
      "s_cselect_b32 s61, s64, s47\n"
      "s_mov_b32 s58, 0\n"
      "s_mov_b32 s59, 0\n"
      "s_cmp_lg_u32 s65, 0\n"
      "s_cselect_b32 s62, s47, s49\n"
      "s_mov_b32 s63, s49\n"
      "s_branch L_issued_%=\n"
      "L_nowork_%=:\n"
      "s_mov_b32 s71, 0\n"
      "L_issued_%=:\n"
      "v_lshlrev_b32 v194, 8, v3\n"
      "v_lshl_add_u32 v12, v190, 4, v194\n"
      "v_add_u32 v12, s34, v12\n"
      "v_xor_b32 v13, 64, v12\n"
      "v_xor_b32 v14, 0x80, v12\n"
      "v_xor_b32 v15, 0xc0, v12\n"
      "v_mov_b32 v185, 0xff800000\n"
      "v_lshlrev_b32 v187, 2, v2\n"
      "v_mov_b32 v188, 0xff800000\n"
      "v_mov_b32 v189, 0\n"
      "v_mov_b32 v176, 0\n"
      "v_mov_b32 v177, 0\n"
      "v_mov_b32 v178, 0\n"
      "v_mov_b32 v179, 0\n"
      "v_mov_b32 v180, 0\n"
      "v_mov_b32 v181, 0\n"
      "v_mov_b32 v182, 0\n"
      "v_mov_b32 v183, 0\n"
      "s_lshl_b32 s64, s28, 2\n"
      "s_add_u32 s64, s64, 147456\n"
      "v_lshl_add_u32 v192, v1, 5, s64\n"
      "v_and_b32 v193, 3, v3\n"
      "v_lshlrev_b32 v193, 5, v193\n"
      "v_add_u32 v193, 147456, v193\n"
      "v_lshrrev_b32 v200, 2, v3\n"
      "v_xor_b32 v200, v200, v2\n"
      "v_lshlrev_b32 v200, 3, v200\n"
      "v_lshl_add_u32 v200, v3, 5, v200\n"
      "s_lshl_b32 s64, s28, 11\n"
      "s_add_u32 s64, s64, 131072\n"
      "v_add_u32 v200, s64, v200\n"
      "v_add_u32 v203, 0x400, v200\n"
      "v_bfe_u32 v201, %2, 5, 2\n"
      "v_bfe_u32 v191, %2, 1, 2\n"
      "v_xor_b32 v201, v201, v191\n"
      "v_and_b32 v191, 0x1f9, %2\n"
      "v_lshl_or_b32 v201, v201, 1, v191\n"
      "v_lshlrev_b32 v201, 2, v201\n"
      "v_add_u32 v201, 131072, v201\n"
      "v_lshrrev_b32 v202, 5, %2\n"
      "v_lshlrev_b32 v202, 12, v202\n"
      "v_and_b32 v191, 31, %2\n"
      "v_lshl_or_b32 v202, v191, 3, v202\n"
      "s_lshl_b32 s64, s29, 16\n"
      "s_add_u32 s64, s64, 4096\n"
      "s_lshl_b32 s65, s30, 8\n"
      "s_add_u32 s65, s65, s64\n"
      "s_add_u32 s72, s20, s65\n"
      "s_addc_u32 s73, s21, 0\n"
      "s_lshl_b32 s65, s30, 12\n"
      "s_add_u32 s65, s65, s64\n"
      "s_add_u32 s74, s20, s65\n"
      "s_addc_u32 s75, s21, 0\n"
      "v_lshrrev_b32 v191, 5, v1\n"
      "v_and_b32 v204, 31, v1\n"
      "v_lshlrev_b32 v204, 3, v204\n"
      "v_lshl_or_b32 v204, v191, 11, v204\n"
      "s_lshr_b32 s25, s30, 2\n"
      "s_lshl2_add_u32 s64, s29, s25\n"
      "s_lshl_b32 s64, s64, 8\n"
      "s_add_u32 s64, s64, 1052672\n"
      "s_add_u32 s22, s20, s64\n"
      "s_addc_u32 s23, s21, 0\n"
      "v_lshl_add_u32 v205, v191, 3, v3\n"
      "v_and_b32 v205, 15, v205\n"
      "v_lshlrev_b32 v205, 3, v205\n"
      "s_lshr_b32 s64, s25, 1\n"
      "s_lshl_b32 s64, s64, 5\n"
      "s_lshl1_add_u32 s64, s29, s64\n"
      "s_and_b32 s65, s25, 1\n"
      "s_add_u32 s64, s64, s65\n"
      "s_lshl_b32 s64, s64, 8\n"
      "s_and_b32 s65, s30, 3\n"
      "s_lshl_b32 s65, s65, 6\n"
      "s_add_u32 s64, s64, s65\n"
      "s_add_u32 s18, s18, s64\n"
      "s_addc_u32 s19, s19, 0\n"
      "v_lshlrev_b32 v206, 1, v1\n"
      "s_and_b32 s64, s28, 3\n"
      "s_lshr_b32 s27, s28, 2\n"
      "s_lshl2_add_u32 s65, s29, s64\n"
      "s_lshl1_add_u32 s65, s65, s27\n"
      "s_lshl_b32 s65, s65, 7\n"
      "s_lshl3_add_u32 s65, s30, s65\n"
      "s_add_u32 s65, s65, 1052672\n"
      "s_add_u32 s42, s20, s65\n"
      "s_addc_u32 s43, s21, 0\n"
      "s_lshl_b64 s[46:47], 1, s64\n"
      "s_mov_b32 s36, 0x05040100\n"
      "s_mov_b32 s37, 0x07060302\n"
      "s_mov_b32 s35, 0x3e0293ee\n"
      "s_mov_b32 vcc_lo, 0xcccccccc\n"
      "s_mov_b32 vcc_hi, 0xcccccccc\n"
      "s_waitcnt vmcnt(0)\n"
      "s_cmp_eq_u32 s54, 0\n"
      "s_cbranch_scc1 L_maxed_%=\n"
      "s_or_b32 s64, s44, s45\n"
      "s_cmp_eq_u32 s64, 0\n"
      "s_cbranch_scc1 L_noflat_%=\n"
      "v_mov_b32 v190, s45\n"
      "v_mov_b32 v191, s44\n"
      "v_cndmask_b32 v190, v191, v190, vcc\n"
      "v_cmp_eq_u32_e64 s[64:65], 0, v190\n"
      "s_nop 1\n"
      "v_cndmask_b32_e64 v16, 0, v16, s[64:65]\n"
      "v_cndmask_b32_e64 v17, 0, v17, s[64:65]\n"
      "v_cndmask_b32_e64 v18, 0, v18, s[64:65]\n"
      "v_cndmask_b32_e64 v19, 0, v19, s[64:65]\n"
      "v_cndmask_b32_e64 v20, 0, v20, s[64:65]\n"
      "v_cndmask_b32_e64 v21, 0, v21, s[64:65]\n"
      "v_cndmask_b32_e64 v22, 0, v22, s[64:65]\n"
      "v_cndmask_b32_e64 v23, 0, v23, s[64:65]\n"
      "v_cndmask_b32_e64 v24, 0, v24, s[64:65]\n"
      "v_cndmask_b32_e64 v25, 0, v25, s[64:65]\n"
      "v_cndmask_b32_e64 v26, 0, v26, s[64:65]\n"
      "v_cndmask_b32_e64 v27, 0, v27, s[64:65]\n"
      "v_cndmask_b32_e64 v28, 0, v28, s[64:65]\n"
      "v_cndmask_b32_e64 v29, 0, v29, s[64:65]\n"
      "v_cndmask_b32_e64 v30, 0, v30, s[64:65]\n"
      "v_cndmask_b32_e64 v31, 0, v31, s[64:65]\n"
      "s_nop 1\n"
      "L_noflat_%=:\n"
      "ds_read_b128 v[96:99], v12\n"
      "ds_read_b128 v[100:103], v13\n"
      "ds_read_b128 v[104:107], v14\n"
      "ds_read_b128 v[108:111], v15\n"
      "s_cmp_lt_u32 s54, 2\n"
      "s_cbranch_scc1 L_rd_%=\n"
      "ds_read_b128 v[112:115], v12 offset:4096\n"
      "ds_read_b128 v[116:119], v13 offset:4096\n"
      "ds_read_b128 v[120:123], v14 offset:4096\n"
      "ds_read_b128 v[124:127], v15 offset:4096\n"
      "s_cmp_lt_u32 s54, 3\n"
      "s_cbranch_scc1 L_rd_%=\n"
      "ds_read_b128 v[128:131], v12 offset:8192\n"
      "ds_read_b128 v[132:135], v13 offset:8192\n"
      "ds_read_b128 v[136:139], v14 offset:8192\n"
      "ds_read_b128 v[140:143], v15 offset:8192\n"
      "s_cmp_lt_u32 s54, 4\n"
      "s_cbranch_scc1 L_rd_%=\n"
      "ds_read_b128 v[144:147], v12 offset:12288\n"
      "ds_read_b128 v[148:151], v13 offset:12288\n"
      "ds_read_b128 v[152:155], v14 offset:12288\n"
      "ds_read_b128 v[156:159], v15 offset:12288\n"
      "L_rd_%=:\n"
      "s_cmp_eq_u32 s54, 1\n"
      "s_cbranch_scc1 L_w0_0_%=\n"
      "s_cmp_eq_u32 s54, 2\n"
      "s_cbranch_scc1 L_w0_4_%=\n"
      "s_cmp_eq_u32 s54, 3\n"
      "s_cbranch_scc1 L_w0_8_%=\n"
      "s_waitcnt lgkmcnt(12)\n"
      "s_branch L_w0_%=\n"
      "L_w0_8_%=:\n"
      "s_waitcnt lgkmcnt(8)\n"
      "s_branch L_w0_%=\n"
      "L_w0_4_%=:\n"
      "s_waitcnt lgkmcnt(4)\n"
      "s_branch L_w0_%=\n"
      "L_w0_0_%=:\n"
      "s_waitcnt lgkmcnt(0)\n"
      "L_w0_%=:\n"
      "v_mfma_f32_16x16x32_bf16 v[160:163], v[96:99], v[16:19], 0\n"
      "v_mfma_f32_16x16x32_bf16 v[160:163], v[100:103], v[20:23], v[160:163]\n"
      "v_mfma_f32_16x16x32_bf16 v[160:163], v[104:107], v[24:27], v[160:163]\n"
      "v_mfma_f32_16x16x32_bf16 v[160:163], v[108:111], v[28:31], v[160:163]\n"
      "s_cmp_lt_u32 s54, 2\n"
      "s_cbranch_scc1 L_mm_%=\n"
      "s_waitcnt lgkmcnt(0)\n"
      "v_mfma_f32_16x16x32_bf16 v[164:167], v[112:115], v[16:19], 0\n"
      "v_mfma_f32_16x16x32_bf16 v[164:167], v[116:119], v[20:23], v[164:167]\n"
      "v_mfma_f32_16x16x32_bf16 v[164:167], v[120:123], v[24:27], v[164:167]\n"
      "v_mfma_f32_16x16x32_bf16 v[164:167], v[124:127], v[28:31], v[164:167]\n"
      "s_cmp_lt_u32 s54, 3\n"
      "s_cbranch_scc1 L_mm_%=\n"
      "v_mfma_f32_16x16x32_bf16 v[168:171], v[128:131], v[16:19], 0\n"
      "v_mfma_f32_16x16x32_bf16 v[168:171], v[132:135], v[20:23], v[168:171]\n"
      "v_mfma_f32_16x16x32_bf16 v[168:171], v[136:139], v[24:27], v[168:171]\n"
      "v_mfma_f32_16x16x32_bf16 v[168:171], v[140:143], v[28:31], v[168:171]\n"
      "s_cmp_lt_u32 s54, 4\n"
      "s_cbranch_scc1 L_mm_%=\n"
      "v_mfma_f32_16x16x32_bf16 v[172:175], v[144:147], v[16:19], 0\n"
      "v_mfma_f32_16x16x32_bf16 v[172:175], v[148:151], v[20:23], v[172:175]\n"
      "v_mfma_f32_16x16x32_bf16 v[172:175], v[152:155], v[24:27], v[172:175]\n"
      "v_mfma_f32_16x16x32_bf16 v[172:175], v[156:159], v[28:31], v[172:175]\n"
      "L_mm_%=:\n"
      "s_nop 7\n"
      "v_mov_b32 v190, s60\n"
      "v_mov_b32 v191, s56\n"
      "v_cndmask_b32 v190, v191, v190, vcc\n"
      "v_sub_u32 v190, v190, v187\n"
      "v_cmp_lt_i32_e64 s[64:65], 0, v190\n"
      "v_cmp_lt_i32_e64 s[66:67], 1, v190\n"
      "v_cmp_lt_i32_e64 s[68:69], 2, v190\n"
      "v_cmp_lt_i32_e64 s[40:41], 3, v190\n"
      "s_nop 0\n"
      "v_cndmask_b32_e64 v160, v185, v160, s[64:65]\n"
      "v_cndmask_b32_e64 v161, v185, v161, s[66:67]\n"
      "v_cndmask_b32_e64 v162, v185, v162, s[68:69]\n"
      "v_cndmask_b32_e64 v163, v185, v163, s[40:41]\n"
      "v_max3_f32 v188, v188, v160, v161\n"
      "v_max3_f32 v188, v188, v162, v163\n"
      "s_cmp_lt_u32 s54, 2\n"
      "s_cbranch_scc1 L_qkdone_%=\n"
      "v_mov_b32 v190, s61\n"
      "v_mov_b32 v191, s57\n"
      "v_cndmask_b32 v190, v191, v190, vcc\n"
      "v_sub_u32 v190, v190, v187\n"
      "v_cmp_lt_i32_e64 s[64:65], 0, v190\n"
      "v_cmp_lt_i32_e64 s[66:67], 1, v190\n"
      "v_cmp_lt_i32_e64 s[68:69], 2, v190\n"
      "v_cmp_lt_i32_e64 s[40:41], 3, v190\n"
      "s_nop 0\n"
      "v_cndmask_b32_e64 v164, v185, v164, s[64:65]\n"
      "v_cndmask_b32_e64 v165, v185, v165, s[66:67]\n"
      "v_cndmask_b32_e64 v166, v185, v166, s[68:69]\n"
      "v_cndmask_b32_e64 v167, v185, v167, s[40:41]\n"
      "v_max3_f32 v188, v188, v164, v165\n"
      "v_max3_f32 v188, v188, v166, v167\n"
      "s_cmp_lt_u32 s54, 3\n"
      "s_cbranch_scc1 L_qkdone_%=\n"
      "v_mov_b32 v190, s62\n"
      "v_mov_b32 v191, s58\n"
      "v_cndmask_b32 v190, v191, v190, vcc\n"
      "v_sub_u32 v190, v190, v187\n"
      "v_cmp_lt_i32_e64 s[64:65], 0, v190\n"
      "v_cmp_lt_i32_e64 s[66:67], 1, v190\n"
      "v_cmp_lt_i32_e64 s[68:69], 2, v190\n"
      "v_cmp_lt_i32_e64 s[40:41], 3, v190\n"
      "s_nop 0\n"
      "v_cndmask_b32_e64 v168, v185, v168, s[64:65]\n"
      "v_cndmask_b32_e64 v169, v185, v169, s[66:67]\n"
      "v_cndmask_b32_e64 v170, v185, v170, s[68:69]\n"
      "v_cndmask_b32_e64 v171, v185, v171, s[40:41]\n"
      "v_max3_f32 v188, v188, v168, v169\n"
      "v_max3_f32 v188, v188, v170, v171\n"
      "s_cmp_lt_u32 s54, 4\n"
      "s_cbranch_scc1 L_qkdone_%=\n"
      "v_mov_b32 v190, s63\n"
      "v_mov_b32 v191, s59\n"
      "v_cndmask_b32 v190, v191, v190, vcc\n"
      "v_sub_u32 v190, v190, v187\n"
      "v_cmp_lt_i32_e64 s[64:65], 0, v190\n"
      "v_cmp_lt_i32_e64 s[66:67], 1, v190\n"
      "v_cmp_lt_i32_e64 s[68:69], 2, v190\n"
      "v_cmp_lt_i32_e64 s[40:41], 3, v190\n"
      "s_nop 0\n"
      "v_cndmask_b32_e64 v172, v185, v172, s[64:65]\n"
      "v_cndmask_b32_e64 v173, v185, v173, s[66:67]\n"
      "v_cndmask_b32_e64 v174, v185, v174, s[68:69]\n"
      "v_cndmask_b32_e64 v175, v185, v175, s[40:41]\n"
      "v_max3_f32 v188, v188, v172, v173\n"
      "v_max3_f32 v188, v188, v174, v175\n"
      "L_qkdone_%=:\n"
      "v_mov_b32 v191, v188\n"
      "s_nop 1\n"
      "v_permlane16_swap_b32 v188, v191\n"
      "s_nop 1\n"
      "v_max_f32 v188, v188, v191\n"
      "v_mov_b32 v191, v188\n"
      "s_nop 1\n"
      "v_permlane32_swap_b32 v188, v191\n"
      "s_nop 1\n"
      "v_max_f32 v188, v188, v191\n"
      "v_mul_f32 v188, s35, v188\n"
      "L_maxed_%=:\n"
      "s_mov_b64 exec, 0xf\n"
      "ds_write_b32 v192, v188\n"
      "s_mov_b64 exec, -1\n"
      "s_cmp_lt_u32 s71, 4\n"
      "s_cbranch_scc1 L_lv3_%=\n"
      "s_cmp_lg_u32 s70, 0\n"
      "s_cbranch_scc1 L_tw13_%=\n"
      "s_waitcnt vmcnt(4)\n"
      "L_tw13_%=:\n"
      "buffer_load_dwordx4 v[80:83], v8, s[8:11], s53 offen nt\n"
      "s_cmp_lg_u32 s70, 0\n"
      "s_cbranch_scc1 L_tw14_%=\n"
      "s_waitcnt vmcnt(4)\n"
      "L_tw14_%=:\n"
      "buffer_load_dwordx4 v[84:87], v9, s[8:11], s53 offen nt\n"
      "s_cmp_lg_u32 s70, 0\n"
      "s_cbranch_scc1 L_tw15_%=\n"
      "s_waitcnt vmcnt(4)\n"
      "L_tw15_%=:\n"
      "buffer_load_dwordx4 v[88:91], v10, s[8:11], s53 offen nt\n"
      "s_cmp_lg_u32 s70, 0\n"
      "s_cbranch_scc1 L_tw16_%=\n"
      "s_waitcnt vmcnt(4)\n"
      "L_tw16_%=:\n"
      "buffer_load_dwordx4 v[92:95], v11, s[8:11], s53 offen nt\n"
      "L_lv3_%=:\n"
      "s_cmp_lt_u32 s71, 3\n"
      "s_cbranch_scc1 L_lv2_%=\n"
      "s_cmp_lg_u32 s70, 0\n"
      "s_cbranch_scc1 L_tw17_%=\n"
      "s_waitcnt vmcnt(4)\n"
      "L_tw17_%=:\n"
      "buffer_load_dwordx4 v[64:67], v8, s[8:11], s52 offen nt\n"
      "s_cmp_lg_u32 s70, 0\n"
      "s_cbranch_scc1 L_tw18_%=\n"
      "s_waitcnt vmcnt(4)\n"
      "L_tw18_%=:\n"
      "buffer_load_dwordx4 v[68:71], v9, s[8:11], s52 offen nt\n"
      "s_cmp_lg_u32 s70, 0\n"
      "s_cbranch_scc1 L_tw19_%=\n"
      "s_waitcnt vmcnt(4)\n"
      "L_tw19_%=:\n"
      "buffer_load_dwordx4 v[72:75], v10, s[8:11], s52 offen nt\n"
      "s_cmp_lg_u32 s70, 0\n"
      "s_cbranch_scc1 L_tw20_%=\n"
      "s_waitcnt vmcnt(4)\n"
      "L_tw20_%=:\n"
      "buffer_load_dwordx4 v[76:79], v11, s[8:11], s52 offen nt\n"
      "L_lv2_%=:\n"
      "s_cmp_lt_u32 s71, 2\n"
      "s_cbranch_scc1 L_lv1_%=\n"
      "s_cmp_lg_u32 s70, 0\n"
      "s_cbranch_scc1 L_tw21_%=\n"
      "s_waitcnt vmcnt(4)\n"
      "L_tw21_%=:\n"
      "buffer_load_dwordx4 v[48:51], v8, s[8:11], s51 offen nt\n"
      "s_cmp_lg_u32 s70, 0\n"
      "s_cbranch_scc1 L_tw22_%=\n"
      "s_waitcnt vmcnt(4)\n"
      "L_tw22_%=:\n"
      "buffer_load_dwordx4 v[52:55], v9, s[8:11], s51 offen nt\n"
      "s_cmp_lg_u32 s70, 0\n"
      "s_cbranch_scc1 L_tw23_%=\n"
      "s_waitcnt vmcnt(4)\n"
      "L_tw23_%=:\n"
      "buffer_load_dwordx4 v[56:59], v10, s[8:11], s51 offen nt\n"
      "s_cmp_lg_u32 s70, 0\n"
      "s_cbranch_scc1 L_tw24_%=\n"
      "s_waitcnt vmcnt(4)\n"
      "L_tw24_%=:\n"
      "buffer_load_dwordx4 v[60:63], v11, s[8:11], s51 offen nt\n"
      "L_lv1_%=:\n"
      "s_cmp_lt_u32 s71, 1\n"
      "s_cbranch_scc1 L_lv0_%=\n"
      "s_cmp_lg_u32 s70, 0\n"
      "s_cbranch_scc1 L_tw25_%=\n"
      "s_waitcnt vmcnt(4)\n"
      "L_tw25_%=:\n"
      "buffer_load_dwordx4 v[32:35], v8, s[8:11], s50 offen nt\n"
      "s_cmp_lg_u32 s70, 0\n"
      "s_cbranch_scc1 L_tw26_%=\n"
      "s_waitcnt vmcnt(4)\n"
      "L_tw26_%=:\n"
      "buffer_load_dwordx4 v[36:39], v9, s[8:11], s50 offen nt\n"
      "s_cmp_lg_u32 s70, 0\n"
      "s_cbranch_scc1 L_tw27_%=\n"
      "s_waitcnt vmcnt(4)\n"
      "L_tw27_%=:\n"
      "buffer_load_dwordx4 v[40:43], v10, s[8:11], s50 offen nt\n"
      "s_cmp_lg_u32 s70, 0\n"
      "s_cbranch_scc1 L_tw28_%=\n"
      "s_waitcnt vmcnt(4)\n"
      "L_tw28_%=:\n"
      "buffer_load_dwordx4 v[44:47], v11, s[8:11], s50 offen nt\n"
      "L_lv0_%=:\n"
      "v_cmp_eq_f32 s[64:65], v188, v185\n"
      "s_mov_b32 s66, 0xf000f0\n"
      "s_mov_b32 s67, 0xf000f0\n"
      "s_mov_b32 s68, 0xf000f00\n"
      "s_mov_b32 s69, 0xf000f00\n"
      "s_mov_b32 s70, 0xf000f000\n"
      "s_mov_b32 s71, 0xf000f000\n"
      "v_cndmask_b32_e64 v197, v188, v196, s[64:65]\n"
      "s_mov_b32 s64, 0x000f000f\n"
      "s_mov_b32 s65, 0x000f000f\n"
      "s_cmp_lt_u32 s54, 1\n"
      "s_cbranch_scc1 L_pdone_%=\n"
      "v_fma_f32 v160, v160, s35, -v197\n"
      "v_fma_f32 v161, v161, s35, -v197\n"
      "v_fma_f32 v162, v162, s35, -v197\n"
      "v_fma_f32 v163, v163, s35, -v197\n"
      "v_exp_f32 v160, v160\n"
      "v_exp_f32 v161, v161\n"
      "v_exp_f32 v162, v162\n"
      "v_exp_f32 v163, v163\n"
      "s_nop 0\n"
      "v_cvt_pk_bf16_f32 v198, v160, v161\n"
      "v_cvt_pk_bf16_f32 v199, v162, v163\n"
      "v_add_f32 v160, v160, v161\n"
      "v_add_f32 v162, v162, v163\n"
      "v_cndmask_b32_e64 v96, 0, v198, s[64:65]\n"
      "v_cndmask_b32_e64 v97, 0, v199, s[64:65]\n"
      "v_cndmask_b32_e64 v98, 0, v198, s[66:67]\n"
      "v_cndmask_b32_e64 v99, 0, v199, s[66:67]\n"
      "v_cndmask_b32_e64 v100, 0, v198, s[68:69]\n"
      "v_cndmask_b32_e64 v101, 0, v199, s[68:69]\n"
      "v_cndmask_b32_e64 v102, 0, v198, s[70:71]\n"
      "v_cndmask_b32_e64 v103, 0, v199, s[70:71]\n"
      "v_add_f32 v189, v189, v160\n"
      "v_add_f32 v189, v189, v162\n"
      "s_cmp_lt_u32 s54, 2\n"
      "s_cbranch_scc1 L_pdone_%=\n"
      "v_fma_f32 v164, v164, s35, -v197\n"
      "v_fma_f32 v165, v165, s35, -v197\n"
      "v_fma_f32 v166, v166, s35, -v197\n"
      "v_fma_f32 v167, v167, s35, -v197\n"
      "v_exp_f32 v164, v164\n"
      "v_exp_f32 v165, v165\n"
      "v_exp_f32 v166, v166\n"
      "v_exp_f32 v167, v167\n"
      "s_nop 0\n"
      "v_cvt_pk_bf16_f32 v198, v164, v165\n"
      "v_cvt_pk_bf16_f32 v199, v166, v167\n"
      "v_add_f32 v164, v164, v165\n"
      "v_add_f32 v166, v166, v167\n"
      "v_cndmask_b32_e64 v104, 0, v198, s[64:65]\n"
      "v_cndmask_b32_e64 v105, 0, v199, s[64:65]\n"
      "v_cndmask_b32_e64 v106, 0, v198, s[66:67]\n"
      "v_cndmask_b32_e64 v107, 0, v199, s[66:67]\n"
      "v_cndmask_b32_e64 v108, 0, v198, s[68:69]\n"
      "v_cndmask_b32_e64 v109, 0, v199, s[68:69]\n"
      "v_cndmask_b32_e64 v110, 0, v198, s[70:71]\n"
      "v_cndmask_b32_e64 v111, 0, v199, s[70:71]\n"
      "v_add_f32 v189, v189, v164\n"
      "v_add_f32 v189, v189, v166\n"
      "s_cmp_lt_u32 s54, 3\n"
      "s_cbranch_scc1 L_pdone_%=\n"
      "v_fma_f32 v168, v168, s35, -v197\n"
      "v_fma_f32 v169, v169, s35, -v197\n"
      "v_fma_f32 v170, v170, s35, -v197\n"
      "v_fma_f32 v171, v171, s35, -v197\n"
      "v_exp_f32 v168, v168\n"
      "v_exp_f32 v169, v169\n"
      "v_exp_f32 v170, v170\n"
      "v_exp_f32 v171, v171\n"
      "s_nop 0\n"
      "v_cvt_pk_bf16_f32 v198, v168, v169\n"
      "v_cvt_pk_bf16_f32 v199, v170, v171\n"
      "v_add_f32 v168, v168, v169\n"
      "v_add_f32 v170, v170, v171\n"
      "v_cndmask_b32_e64 v112, 0, v198, s[64:65]\n"
      "v_cndmask_b32_e64 v113, 0, v199, s[64:65]\n"
      "v_cndmask_b32_e64 v114, 0, v198, s[66:67]\n"
      "v_cndmask_b32_e64 v115, 0, v199, s[66:67]\n"
      "v_cndmask_b32_e64 v116, 0, v198, s[68:69]\n"
      "v_cndmask_b32_e64 v117, 0, v199, s[68:69]\n"
      "v_cndmask_b32_e64 v118, 0, v198, s[70:71]\n"
      "v_cndmask_b32_e64 v119, 0, v199, s[70:71]\n"
      "v_add_f32 v189, v189, v168\n"
      "v_add_f32 v189, v189, v170\n"
      "s_cmp_lt_u32 s54, 4\n"
      "s_cbranch_scc1 L_pdone_%=\n"
      "v_fma_f32 v172, v172, s35, -v197\n"
      "v_fma_f32 v173, v173, s35, -v197\n"
      "v_fma_f32 v174, v174, s35, -v197\n"
      "v_fma_f32 v175, v175, s35, -v197\n"
      "v_exp_f32 v172, v172\n"
      "v_exp_f32 v173, v173\n"
      "v_exp_f32 v174, v174\n"
      "v_exp_f32 v175, v175\n"
      "s_nop 0\n"
      "v_cvt_pk_bf16_f32 v198, v172, v173\n"
      "v_cvt_pk_bf16_f32 v199, v174, v175\n"
      "v_add_f32 v172, v172, v173\n"
      "v_add_f32 v174, v174, v175\n"
      "v_cndmask_b32_e64 v120, 0, v198, s[64:65]\n"
      "v_cndmask_b32_e64 v121, 0, v199, s[64:65]\n"
      "v_cndmask_b32_e64 v122, 0, v198, s[66:67]\n"
      "v_cndmask_b32_e64 v123, 0, v199, s[66:67]\n"
      "v_cndmask_b32_e64 v124, 0, v198, s[68:69]\n"
      "v_cndmask_b32_e64 v125, 0, v199, s[68:69]\n"
      "v_cndmask_b32_e64 v126, 0, v198, s[70:71]\n"
      "v_cndmask_b32_e64 v127, 0, v199, s[70:71]\n"
      "v_add_f32 v189, v189, v172\n"
      "v_add_f32 v189, v189, v174\n"
      "L_pdone_%=:\n"
      "v_mov_b32 v191, v189\n"
      "s_nop 1\n"
      "v_permlane16_swap_b32 v189, v191\n"
      "s_nop 1\n"
      "v_add_f32 v189, v189, v191\n"
      "v_mov_b32 v191, v189\n"
      "s_nop 1\n"
      "v_permlane32_swap_b32 v189, v191\n"
      "s_nop 1\n"
      "v_add_f32 v189, v189, v191\n"
      "s_mov_b64 exec, 0xf\n"
      "ds_write_b32 v192, v189 offset:128\n"
      "s_mov_b64 exec, -1\n"
      "v_add_u32 v184, 0x9e3779b1, v184\n"
      "s_waitcnt lgkmcnt(0)\n"
      "s_barrier\n"
      "ds_read_b128 v[144:147], v193\n"
      "ds_read_b128 v[148:151], v193 offset:16\n"
      "ds_read_b128 v[152:155], v193 offset:128\n"
      "ds_read_b128 v[156:159], v193 offset:144\n"
      "s_waitcnt lgkmcnt(2)\n"
      "v_max3_f32 v190, v144, v145, v146\n"
      "v_max3_f32 v190, v190, v147, v148\n"
      "v_max3_f32 v190, v190, v149, v150\n"
      "v_max_f32 v190, v190, v151\n"
      "v_cmp_eq_f32 s[64:65], v190, v185\n"
      "s_nop 1\n"
      "v_cndmask_b32_e64 v191, v190, v196, s[64:65]\n"
      "v_sub_f32 v188, v188, v191\n"
      "v_sub_f32 v144, v144, v191\n"
      "v_sub_f32 v145, v145, v191\n"
      "v_sub_f32 v146, v146, v191\n"
      "v_sub_f32 v147, v147, v191\n"
      "v_sub_f32 v148, v148, v191\n"
      "v_sub_f32 v149, v149, v191\n"
      "v_sub_f32 v150, v150, v191\n"
      "v_sub_f32 v151, v151, v191\n"
      "v_exp_f32 v188, v188\n"
      "v_exp_f32 v144, v144\n"
      "v_exp_f32 v145, v145\n"
      "v_exp_f32 v146, v146\n"
      "v_exp_f32 v147, v147\n"
      "v_exp_f32 v148, v148\n"
      "v_exp_f32 v149, v149\n"
      "v_exp_f32 v150, v150\n"
      "v_exp_f32 v151, v151\n"
      "s_waitcnt lgkmcnt(0)\n"
      "v_mul_f32 v144, v144, v152\n"
      "v_fmac_f32 v144, v145, v153\n"
      "v_fmac_f32 v144, v146, v154\n"
      "v_fmac_f32 v144, v147, v155\n"
      "v_fmac_f32 v144, v148, v156\n"
      "v_fmac_f32 v144, v149, v157\n"
      "v_fmac_f32 v144, v150, v158\n"
      "v_fmac_f32 v144, v151, v159\n"
      "v_mov_b32_dpp v208, v188 quad_perm:[0,0,0,0] row_mask:0xf bank_mask:0xf\n"
      "v_mov_b32_dpp v209, v188 quad_perm:[1,1,1,1] row_mask:0xf bank_mask:0xf\n"
      "v_mov_b32_dpp v210, v188 quad_perm:[2,2,2,2] row_mask:0xf bank_mask:0xf\n"
      "v_mov_b32_dpp v211, v188 quad_perm:[3,3,3,3] row_mask:0xf bank_mask:0xf\n"
      "s_cmp_eq_u32 s27, 0\n"
      "s_cselect_b64 vcc, -1, 0\n"
      "v_cndmask_b32 v212, v144, v190, vcc\n"
      "v_mov_b32 v213, v184\n"
      "s_mov_b64 exec, s[46:47]\n"
      "global_store_dwordx2 v196, v[212:213], s[42:43] sc1\n"
      "s_mov_b64 exec, -1\n"
      "s_cmp_lt_u32 s54, 4\n"
      "s_cbranch_scc1 L_pv3_%=\n"
      "s_waitcnt vmcnt(13)\n"
      "v_perm_b32 v128, v84, v80, s36\n"
      "v_perm_b32 v129, v92, v88, s36\n"
      "v_perm_b32 v130, v84, v80, s37\n"
      "v_perm_b32 v131, v92, v88, s37\n"
      "v_perm_b32 v132, v85, v81, s36\n"
      "v_perm_b32 v133, v93, v89, s36\n"
      "v_perm_b32 v134, v85, v81, s37\n"
      "v_perm_b32 v135, v93, v89, s37\n"
      "v_mfma_f32_16x16x16_bf16 v[176:179], v[120:121], v[128:129], v[176:179]\n"
      "v_mfma_f32_16x16x16_bf16 v[180:183], v[120:121], v[130:131], v[180:183]\n"
      "v_perm_b32 v136, v86, v82, s36\n"
      "v_perm_b32 v137, v94, v90, s36\n"
      "v_perm_b32 v138, v86, v82, s37\n"
      "v_perm_b32 v139, v94, v90, s37\n"
      "v_mfma_f32_16x16x16_bf16 v[176:179], v[122:123], v[132:133], v[176:179]\n"
      "v_mfma_f32_16x16x16_bf16 v[180:183], v[122:123], v[134:135], v[180:183]\n"
      "v_perm_b32 v140, v87, v83, s36\n"
      "v_perm_b32 v141, v95, v91, s36\n"
      "v_perm_b32 v142, v87, v83, s37\n"
      "v_perm_b32 v143, v95, v91, s37\n"
      "v_mfma_f32_16x16x16_bf16 v[176:179], v[124:125], v[136:137], v[176:179]\n"
      "v_mfma_f32_16x16x16_bf16 v[180:183], v[124:125], v[138:139], v[180:183]\n"
      "s_nop 1\n"
      "v_mfma_f32_16x16x16_bf16 v[176:179], v[126:127], v[140:141], v[176:179]\n"
      "v_mfma_f32_16x16x16_bf16 v[180:183], v[126:127], v[142:143], v[180:183]\n"
      "L_pv3_%=:\n"
      "s_cmp_lt_u32 s54, 3\n"
      "s_cbranch_scc1 L_pv2_%=\n"
      "s_waitcnt vmcnt(9)\n"
      "v_perm_b32 v128, v68, v64, s36\n"
      "v_perm_b32 v129, v76, v72, s36\n"
      "v_perm_b32 v130, v68, v64, s37\n"
      "v_perm_b32 v131, v76, v72, s37\n"
      "v_perm_b32 v132, v69, v65, s36\n"
      "v_perm_b32 v133, v77, v73, s36\n"
      "v_perm_b32 v134, v69, v65, s37\n"
      "v_perm_b32 v135, v77, v73, s37\n"
      "v_mfma_f32_16x16x16_bf16 v[176:179], v[112:113], v[128:129], v[176:179]\n"
      "v_mfma_f32_16x16x16_bf16 v[180:183], v[112:113], v[130:131], v[180:183]\n"
      "v_perm_b32 v136, v70, v66, s36\n"
      "v_perm_b32 v137, v78, v74, s36\n"
      "v_perm_b32 v138, v70, v66, s37\n"
      "v_perm_b32 v139, v78, v74, s37\n"
      "v_mfma_f32_16x16x16_bf16 v[176:179], v[114:115], v[132:133], v[176:179]\n"
      "v_mfma_f32_16x16x16_bf16 v[180:183], v[114:115], v[134:135], v[180:183]\n"
      "v_perm_b32 v140, v71, v67, s36\n"
      "v_perm_b32 v141, v79, v75, s36\n"
      "v_perm_b32 v142, v71, v67, s37\n"
      "v_perm_b32 v143, v79, v75, s37\n"
      "v_mfma_f32_16x16x16_bf16 v[176:179], v[116:117], v[136:137], v[176:179]\n"
      "v_mfma_f32_16x16x16_bf16 v[180:183], v[116:117], v[138:139], v[180:183]\n"
      "s_nop 1\n"
      "v_mfma_f32_16x16x16_bf16 v[176:179], v[118:119], v[140:141], v[176:179]\n"
      "v_mfma_f32_16x16x16_bf16 v[180:183], v[118:119], v[142:143], v[180:183]\n"
      "L_pv2_%=:\n"
      "s_cmp_lt_u32 s54, 2\n"
      "s_cbranch_scc1 L_pv1_%=\n"
      "s_waitcnt vmcnt(5)\n"
      "v_perm_b32 v128, v52, v48, s36\n"
      "v_perm_b32 v129, v60, v56, s36\n"
      "v_perm_b32 v130, v52, v48, s37\n"
      "v_perm_b32 v131, v60, v56, s37\n"
      "v_perm_b32 v132, v53, v49, s36\n"
      "v_perm_b32 v133, v61, v57, s36\n"
      "v_perm_b32 v134, v53, v49, s37\n"
      "v_perm_b32 v135, v61, v57, s37\n"
      "v_mfma_f32_16x16x16_bf16 v[176:179], v[104:105], v[128:129], v[176:179]\n"
      "v_mfma_f32_16x16x16_bf16 v[180:183], v[104:105], v[130:131], v[180:183]\n"
      "v_perm_b32 v136, v54, v50, s36\n"
      "v_perm_b32 v137, v62, v58, s36\n"
      "v_perm_b32 v138, v54, v50, s37\n"
      "v_perm_b32 v139, v62, v58, s37\n"
      "v_mfma_f32_16x16x16_bf16 v[176:179], v[106:107], v[132:133], v[176:179]\n"
      "v_mfma_f32_16x16x16_bf16 v[180:183], v[106:107], v[134:135], v[180:183]\n"
      "v_perm_b32 v140, v55, v51, s36\n"
      "v_perm_b32 v141, v63, v59, s36\n"
      "v_perm_b32 v142, v55, v51, s37\n"
      "v_perm_b32 v143, v63, v59, s37\n"
      "v_mfma_f32_16x16x16_bf16 v[176:179], v[108:109], v[136:137], v[176:179]\n"
      "v_mfma_f32_16x16x16_bf16 v[180:183], v[108:109], v[138:139], v[180:183]\n"
      "s_nop 1\n"
      "v_mfma_f32_16x16x16_bf16 v[176:179], v[110:111], v[140:141], v[176:179]\n"
      "v_mfma_f32_16x16x16_bf16 v[180:183], v[110:111], v[142:143], v[180:183]\n"
      "L_pv1_%=:\n"
      "s_cmp_lt_u32 s54, 1\n"
      "s_cbranch_scc1 L_pv0_%=\n"
      "s_waitcnt vmcnt(1)\n"
      "v_perm_b32 v128, v36, v32, s36\n"
      "v_perm_b32 v129, v44, v40, s36\n"
      "v_perm_b32 v130, v36, v32, s37\n"
      "v_perm_b32 v131, v44, v40, s37\n"
      "v_perm_b32 v132, v37, v33, s36\n"
      "v_perm_b32 v133, v45, v41, s36\n"
      "v_perm_b32 v134, v37, v33, s37\n"
      "v_perm_b32 v135, v45, v41, s37\n"
      "v_mfma_f32_16x16x16_bf16 v[176:179], v[96:97], v[128:129], v[176:179]\n"
      "v_mfma_f32_16x16x16_bf16 v[180:183], v[96:97], v[130:131], v[180:183]\n"
      "v_perm_b32 v136, v38, v34, s36\n"
      "v_perm_b32 v137, v46, v42, s36\n"
      "v_perm_b32 v138, v38, v34, s37\n"
      "v_perm_b32 v139, v46, v42, s37\n"
      "v_mfma_f32_16x16x16_bf16 v[176:179], v[98:99], v[132:133], v[176:179]\n"
      "v_mfma_f32_16x16x16_bf16 v[180:183], v[98:99], v[134:135], v[180:183]\n"
      "v_perm_b32 v140, v39, v35, s36\n"
      "v_perm_b32 v141, v47, v43, s36\n"
      "v_perm_b32 v142, v39, v35, s37\n"
      "v_perm_b32 v143, v47, v43, s37\n"
      "v_mfma_f32_16x16x16_bf16 v[176:179], v[100:101], v[136:137], v[176:179]\n"
      "v_mfma_f32_16x16x16_bf16 v[180:183], v[100:101], v[138:139], v[180:183]\n"
      "s_nop 1\n"
      "v_mfma_f32_16x16x16_bf16 v[176:179], v[102:103], v[140:141], v[176:179]\n"
      "v_mfma_f32_16x16x16_bf16 v[180:183], v[102:103], v[142:143], v[180:183]\n"
      "L_pv0_%=:\n"
      "s_nop 7\n"
      "v_mul_f32 v176, v176, v208\n"
      "v_mul_f32 v180, v180, v208\n"
      "v_mul_f32 v177, v177, v209\n"
      "v_mul_f32 v181, v181, v209\n"
      "v_mul_f32 v178, v178, v210\n"
      "v_mul_f32 v182, v182, v210\n"
      "v_mul_f32 v179, v179, v211\n"
      "v_mul_f32 v183, v183, v211\n"
      "ds_write2_b32 v200, v176, v180 offset0:0 offset1:1\n"
      "ds_write2_b32 v200, v177, v181 offset0:128 offset1:129\n"
      "ds_write2_b32 v203, v178, v182 offset0:0 offset1:1\n"
      "ds_write2_b32 v203, v179, v183 offset0:128 offset1:129\n"
      "s_cmp_lg_u32 s28, 7\n"
      "s_cbranch_scc1 L_noml_%=\n"
      "global_load_dwordx2 v[48:49], v205, s[22:23] sc1\n"
      "global_load_dwordx2 v[50:51], v205, s[22:23] offset:128 sc1\n"
      "L_noml_%=:\n"
      "s_waitcnt lgkmcnt(0)\n"
      "s_barrier\n"
      "ds_read2st64_b32 v[144:145], v201 offset0:0 offset1:8\n"
      "ds_read2st64_b32 v[146:147], v201 offset0:16 offset1:24\n"
      "ds_read2st64_b32 v[148:149], v201 offset0:32 offset1:40\n"
      "ds_read2st64_b32 v[150:151], v201 offset0:48 offset1:56\n"
      "v_mov_b32 v213, v184\n"
      "s_waitcnt lgkmcnt(2)\n"
      "v_add_f32 v144, v144, v145\n"
      "v_add_f32 v146, v146, v147\n"
      "s_waitcnt lgkmcnt(0)\n"
      "v_add_f32 v148, v148, v149\n"
      "v_add_f32 v150, v150, v151\n"
      "v_add_f32 v144, v144, v146\n"
      "v_add_f32 v148, v148, v150\n"
      "v_add_f32 v212, v144, v148\n"
      "global_store_dwordx2 v202, v[212:213], s[72:73] sc1\n"
      "s_cmp_lg_u32 s28, 7\n"
      "s_cbranch_scc1 L_end_%=\n"
      "s_mov_b32 s24, 0\n"
      "s_waitcnt vmcnt(1)\n"
      "s_branch L_poll1c_%=\n"
      "L_poll1_%=:\n"
      "global_load_dwordx2 v[48:49], v205, s[22:23] sc1\n"
      "global_load_dwordx2 v[50:51], v205, s[22:23] offset:128 sc1\n"
      "L_poll1w_%=:\n"
      "s_waitcnt vmcnt(0)\n"
      "L_poll1c_%=:\n"
      "v_cmpx_eq_u32 vcc, v49, v184\n"
      "v_cmpx_eq_u32 vcc, v51, v184\n"
      "s_cmp_eq_u64 exec, -1\n"
      "s_mov_b64 exec, -1\n"
      "s_cbranch_scc1 L_got1_%=\n"
      "s_add_u32 s24, s24, 1\n"
      "s_cmp_lt_u32 s24, 0x400000\n"
      "s_cbranch_scc1 L_poll1_%=\n"
      "v_mov_b32 v48, 0x7fc00000\n"
      "L_got1_%=:\n"
      "v_mov_b32 v52, v48\n"
      "s_nop 1\n"
      "v_max_f32_dpp v52, v52, v52 row_ror:8 row_mask:0xf bank_mask:0xf\n"
      "s_nop 1\n"
      "v_max_f32_dpp v52, v52, v52 row_ror:4 row_mask:0xf bank_mask:0xf\n"
      "s_nop 1\n"
      "v_max_f32_dpp v52, v52, v52 row_ror:2 row_mask:0xf bank_mask:0xf\n"
      "s_nop 1\n"
      "v_max_f32_dpp v52, v52, v52 row_ror:1 row_mask:0xf bank_mask:0xf\n"
      "v_sub_f32 v53, v48, v52\n"
      "v_exp_f32 v53, v53\n"
      "s_nop 0\n"
      "v_mul_f32 v54, v53, v50\n"
      "s_nop 1\n"
      "v_add_f32_dpp v54, v54, v54 row_ror:8 row_mask:0xf bank_mask:0xf\n"
      "s_nop 1\n"
      "v_add_f32_dpp v54, v54, v54 row_ror:4 row_mask:0xf bank_mask:0xf\n"
      "s_nop 1\n"
      "v_add_f32_dpp v54, v54, v54 row_ror:2 row_mask:0xf bank_mask:0xf\n"
      "s_nop 1\n"
      "v_add_f32_dpp v54, v54, v54 row_ror:1 row_mask:0xf bank_mask:0xf\n"
      "v_rcp_f32 v54, v54\n"
      "s_nop 0\n"
      "v_mul_f32 v53, v53, v54\n"
      "s_waitcnt vmcnt(0)\n"
      "global_load_dwordx2 v[32:33], v204, s[74:75] sc1\n"
      "global_load_dwordx2 v[34:35], v204, s[74:75] offset:256 sc1\n"
      "global_load_dwordx2 v[36:37], v204, s[74:75] offset:512 sc1\n"
      "global_load_dwordx2 v[38:39], v204, s[74:75] offset:768 sc1\n"
      "global_load_dwordx2 v[40:41], v204, s[74:75] offset:1024 sc1\n"
      "global_load_dwordx2 v[42:43], v204, s[74:75] offset:1280 sc1\n"
      "global_load_dwordx2 v[44:45], v204, s[74:75] offset:1536 sc1\n"
      "global_load_dwordx2 v[46:47], v204, s[74:75] offset:1792 sc1\n"
      "s_mov_b32 s24, 0\n"
      "L_poll2_%=:\n"
      "s_waitcnt vmcnt(0)\n"
      "v_cmpx_eq_u32 vcc, v33, v184\n"
      "v_cmpx_eq_u32 vcc, v35, v184\n"
      "v_cmpx_eq_u32 vcc, v37, v184\n"
      "v_cmpx_eq_u32 vcc, v39, v184\n"
      "v_cmpx_eq_u32 vcc, v41, v184\n"
      "v_cmpx_eq_u32 vcc, v43, v184\n"
      "v_cmpx_eq_u32 vcc, v45, v184\n"
      "v_cmpx_eq_u32 vcc, v47, v184\n"
      "s_cmp_eq_u64 exec, -1\n"
      "s_mov_b64 exec, -1\n"
      "s_cbranch_scc1 L_got2_0_%=\n"
      "global_load_dwordx2 v[32:33], v204, s[74:75] sc1\n"
      "global_load_dwordx2 v[34:35], v204, s[74:75] offset:256 sc1\n"
      "global_load_dwordx2 v[36:37], v204, s[74:75] offset:512 sc1\n"
      "global_load_dwordx2 v[38:39], v204, s[74:75] offset:768 sc1\n"
      "global_load_dwordx2 v[40:41], v204, s[74:75] offset:1024 sc1\n"
      "global_load_dwordx2 v[42:43], v204, s[74:75] offset:1280 sc1\n"
      "global_load_dwordx2 v[44:45], v204, s[74:75] offset:1536 sc1\n"
      "global_load_dwordx2 v[46:47], v204, s[74:75] offset:1792 sc1\n"
      "s_add_u32 s24, s24, 1\n"
      "s_cmp_lt_u32 s24, 0x200000\n"
      "s_cbranch_scc1 L_poll2_%=\n"
      "s_waitcnt vmcnt(0)\n"
      "v_mov_b32 v32, 0x7fc00000\n"
      "L_got2_0_%=:\n"
      "s_nop 4\n"
      "v_mul_f32_dpp v55, v53, v32 row_newbcast:0 row_mask:0xf bank_mask:0xf\n"
      "v_mul_f32_dpp v56, v53, v34 row_newbcast:1 row_mask:0xf bank_mask:0xf\n"
      "v_fmac_f32_dpp v55, v53, v36 row_newbcast:2 row_mask:0xf bank_mask:0xf\n"
      "v_fmac_f32_dpp v56, v53, v38 row_newbcast:3 row_mask:0xf bank_mask:0xf\n"
      "v_fmac_f32_dpp v55, v53, v40 row_newbcast:4 row_mask:0xf bank_mask:0xf\n"
      "v_fmac_f32_dpp v56, v53, v42 row_newbcast:5 row_mask:0xf bank_mask:0xf\n"
      "v_fmac_f32_dpp v55, v53, v44 row_newbcast:6 row_mask:0xf bank_mask:0xf\n"
      "v_fmac_f32_dpp v56, v53, v46 row_newbcast:7 row_mask:0xf bank_mask:0xf\n"
      "L_fin2_%=:\n"
      "v_add_f32 v55, v55, v56\n"
      "v_mov_b32 v56, v55\n"
      "s_nop 1\n"
      "v_permlane32_swap_b32 v55, v56\n"
      "s_nop 1\n"
      "v_add_f32 v55, v55, v56\n"
      "v_cvt_pk_bf16_f32 v55, v55, v55\n"
      "s_mov_b32 exec_hi, 0\n"
      "global_store_short v206, v55, s[18:19]\n"
      "s_cmp_lg_u32 s30, 0\n"
      "s_cbranch_scc1 L_end_%=\n"
      "s_add_u32 s66, s20, s33\n"
      "s_addc_u32 s67, s21, 0\n"
      "s_mov_b64 exec, 1\n"
      "global_store_dword v196, v184, s[66:67]\n"
      "L_end_%=:\n"
      "s_endpgm\n"
      :
      : "s"(__builtin_amdgcn_kernarg_segment_ptr()), "s"(blockIdx.x), "v"(threadIdx.x), "s"((unsigned)(uintptr_t)(__attribute__((address_space(3))) unsigned char*)lds)
      : "v1", "v2", "v3", "v4", "v5", "v6", "v7", "v8", "v9", "v10", "v11", "v12", "v13", "v14", "v15", "v16", "v17", "v18", "v19", "v20", "v21", "v22", "v23", "v24", "v25", "v26", "v27", "v28", "v29", "v30", "v31", "v32", "v33", "v34", "v35", "v36", "v37", "v38", "v39", "v40", "v41", "v42", "v43", "v44", "v45", "v46", "v47", "v48", "v49", "v50", "v51", "v52", "v53", "v54", "v55", "v56", "v57", "v58", "v59", "v60", "v61", "v62", "v63", "v64", "v65", "v66", "v67", "v68", "v69", "v70", "v71", "v72", "v73", "v74", "v75", "v76", "v77", "v78", "v79", "v80", "v81", "v82", "v83", "v84", "v85", "v86", "v87", "v88", "v89", "v90", "v91", "v92", "v93", "v94", "v95", "v96", "v97", "v98", "v99", "v100", "v101", "v102", "v103", "v104", "v105", "v106", "v107", "v108", "v109", "v110", "v111", "v112", "v113", "v114", "v115", "v116", "v117", "v118", "v119", "v120", "v121", "v122", "v123", "v124", "v125", "v126", "v127", "v128", "v129", "v130", "v131", "v132", "v133", "v134", "v135", "v136", "v137", "v138", "v139", "v140", "v141", "v142", "v143", "v144", "v145", "v146", "v147", "v148", "v149", "v150", "v151", "v152", "v153", "v154", "v155", "v156", "v157", "v158", "v159", "v160", "v161", "v162", "v163", "v164", "v165", "v166", "v167", "v168", "v169", "v170", "v171", "v172", "v173", "v174", "v175", "v176", "v177", "v178", "v179", "v180", "v181", "v182", "v183", "v184", "v185", "v186", "v187", "v188", "v189", "v190", "v191", "v192", "v193", "v194", "v195", "v196", "v197", "v198", "v199", "v200", "v201", "v202", "v203", "v204", "v205", "v206", "v207", "v208", "v209", "v210", "v211", "v212", "v213", "v214", "v215", "v216", "v217", "v218", "v219",
        "s4", "s5", "s6", "s7", "s8", "s9", "s10", "s11", "s12", "s13", "s14", "s15", "s16", "s17", "s18", "s19", "s20", "s21", "s22", "s23", "s24", "s25", "s26", "s27", "s28", "s29", "s30", "s31", "s33", "s34", "s35", "s36", "s37", "s38", "s39", "s40", "s41", "s42", "s43", "s44", "s45", "s46", "s47", "s48", "s49", "s50", "s51", "s52", "s53", "s54", "s55", "s56", "s57", "s58", "s59", "s60", "s61", "s62", "s63", "s64", "s65", "s66", "s67", "s68", "s69", "s70", "s71", "s72", "s73", "s74", "s75", "vcc", "scc", "memory");
#endif
}

extern "C" void kernel_launch(void* const* d_in, const int* in_sizes, int n_in, void* d_out, int out_size, void* d_ws, size_t ws_size,
                              hipStream_t stream) {
  const unsigned kv_bytes = (unsigned)in_sizes[1] * 2u;
  attn_decode<<<256, 512, 0, stream>>>(d_in[1], kv_bytes, 0x20000u, d_in[2], kv_bytes, 0x20000u, d_in[0], d_in[3], d_in[4], d_out, d_ws);
}
```
